```python
import torch

def _softmax(x, axis=-1):
    return torch.softmax(x, dim=axis)

def _gelu(x):
    return torch.nn.functional.gelu(x, approximate='tanh')

def _silu(x):
    return torch.nn.functional.silu(x)

def _sigmoid(x):
    return torch.sigmoid(x)

def _layernorm(x, axis=-1, eps=1e-05):
    m = x.mean(dim=axis, keepdim=True)
    v = x.var(dim=axis, keepdim=True, unbiased=False)
    return (x - m) / torch.sqrt(v + eps)

def _scatter_add(tgt, idx, src, axis=0):
    return tgt.clone().index_add_(axis, idx, src)

def _segment_sum(src, seg, n):
    out = torch.zeros((n,) + src.shape[1:], dtype=src.dtype)
    return out.index_add_(0, seg, src)

def _pad(x, pw):
    flat = tuple((v for pair in reversed(pw) for v in pair))
    return torch.nn.functional.pad(x, flat)
kernel_name = 'recurrent_scan__mha_block_cached__mixer_gmlp__prompt'

def setup_inputs():

    _w = lambda a: (lambda t: t.to(torch.bfloat16) if t.is_floating_point() else t)(torch.as_tensor(a))

    def _A_setup():
        B, Sq, D = (1, 2048, 640)
        H, Sk, Dh = (32, 2048, 128)
        return {'x': _w((lambda *s: torch.randn(*s, dtype=torch.bfloat16) * 0.05)(B, Sq, D)), 'k_cache': _w((lambda *s: torch.randn(*s, dtype=torch.bfloat16) * 0.8)(B, H, Sk, Dh)), 'v_cache': _w((lambda *s: torch.randn(*s, dtype=torch.bfloat16) * 0.2)(B, H, Sk, Dh)), 'wq': _w((lambda *s: torch.randn(*s, dtype=torch.bfloat16) * 0.8)(D, H * Dh) * D ** (-0.5)), 'wo': _w((lambda *s: torch.randn(*s, dtype=torch.bfloat16) * 0.2)(H * Dh, D) * (H * Dh) ** (-0.5))}
    _A_in = _A_setup()

    def _B_setup():
        B, S, D, F = (1, 2048, 640, 1728)
        Fh = F // 2
        return {'x': _w((lambda *s: torch.randn(*s, dtype=torch.bfloat16) * 0.05)(B, S, D)), 'w_in': _w((lambda *s: torch.randn(*s, dtype=torch.bfloat16) * 0.4)(D, 2 * Fh)), 'w_sp': _w(0.01 * (lambda *s: torch.randn(*s, dtype=torch.bfloat16) * 0.4)(S, S)), 'b_sp': _w(1.0 + 0.01 * (lambda *s: torch.randn(*s, dtype=torch.bfloat16) * 0.1)(S)), 'w_out': _w((lambda *s: torch.randn(*s, dtype=torch.bfloat16) * 0.1)(Fh, D))}
    _B_in = _B_setup()
    return {'A_x': _A_in['x'], 'A_k_cache': _A_in['k_cache'], 'A_v_cache': _A_in['v_cache'], 'A_wq': _A_in['wq'], 'A_wo': _A_in['wo'], 'B_x': _B_in['x'], 'B_w_in': _B_in['w_in'], 'B_w_sp': _B_in['w_sp'], 'B_b_sp': _B_in['b_sp'], 'B_w_out': _B_in['w_out']}

def reference(A_x, A_k_cache, A_v_cache, A_wq, A_wo, B_x, B_w_in, B_w_sp, B_b_sp, B_w_out):

    def _A_ref(x, k_cache, v_cache, wq, wo):
        B, Sq, D = x.shape
        H, Dh = (32, 128)
        q = (lambda x, *d: x.permute(*d))((x @ wq).reshape(B, Sq, H, Dh), 0, 2, 1, 3)
        scores = torch.einsum('bhqd,bhkd->bhqk', q, k_cache) * Dh ** (-0.5)
        p = _softmax(scores, axis=-1)
        o = torch.einsum('bhqk,bhkd->bhqd', p, v_cache)
        return (lambda x, *d: x.permute(*d))(o, 0, 2, 1, 3).reshape(B, Sq, H * Dh) @ wo

    def _B_ref(x, w_in, w_sp, b_sp, w_out):
        B, S, D = x.shape
        F = w_in.shape[-1]
        Fh = F // 2
        z = _gelu(torch.einsum('bsd,df->bsf', x, w_in))
        z1 = z[..., :Fh].clone()
        z2 = _layernorm(z[..., Fh:], axis=-1)
        g = torch.einsum('ts,bsf->btf', w_sp, z2) + b_sp[None, :, None]
        return torch.einsum('bsf,fd->bsd', z1 * g, w_out)
    x = A_x
    xn = _layernorm(x, axis=-1)
    s = _A_ref(xn, A_k_cache, A_v_cache, A_wq, A_wo)
    g = _silu(_B_ref(xn, B_w_in, B_w_sp, B_b_sp, B_w_out))
    return x + s * g
```

```cpp
#include <hip/hip_runtime.h>
#include <hip/hip_bf16.h>
#include <utility>

typedef float    f32x16 __attribute__((ext_vector_type(16)));
typedef float    f32x2  __attribute__((ext_vector_type(2)));
typedef float    f32x4  __attribute__((ext_vector_type(4)));
typedef __bf16   bf16x8 __attribute__((ext_vector_type(8)));
typedef __bf16   bf16x4 __attribute__((ext_vector_type(4)));
typedef __bf16   bf16x2 __attribute__((ext_vector_type(2)));
typedef unsigned u32x4  __attribute__((ext_vector_type(4)));
typedef unsigned u32x2  __attribute__((ext_vector_type(2)));
template <int... Is> using iseq = std::integer_sequence<int, Is...>;
template <int N> using make_iseq = std::make_integer_sequence<int, N>;
typedef __amdgpu_buffer_rsrc_t rsrc_t;

#define DI __device__ __forceinline__
#define GAP() __builtin_amdgcn_sched_barrier(0)
#define SRD_WORD3 0x00020000
#define A10(x) "a" #x "0", "a" #x "1", "a" #x "2", "a" #x "3", "a" #x "4", "a" #x "5", "a" #x "6", "a" #x "7", "a" #x "8", "a" #x "9"
#define ACLOB_LO "a0", "a1", "a2", "a3", "a4", "a5", "a6", "a7", "a8", "a9", A10(1), A10(2), A10(3), A10(4), A10(5), A10(6), A10(7), A10(8), A10(9), A10(10), A10(11), \
                 "a120", "a121", "a122", "a123", "a124", "a125", "a126", "a127"
#define ACLOB_HI "a128", "a129", A10(13), A10(14), A10(15), A10(16), A10(17), A10(18), A10(19), A10(20), A10(21), A10(22), A10(23), A10(24), \
                 "a250", "a251", "a252", "a253", "a254", "a255"
#define ACLOB ACLOB_LO, ACLOB_HI

constexpr int SEQ = 2048, DM = 640, NH = 32, DH = 128, FF = 1728, FH = 864;
constexpr size_t WS_XN  = 0;
constexpr size_t WS_Z   = WS_XN  + (size_t)SEQ * DM * 2;
constexpr size_t WS_O   = WS_Z   + (size_t)SEQ * FF * 2;
constexpr size_t WS_Z2N = WS_O   + (size_t)SEQ * NH * DH * 2;
constexpr size_t WS_WSP = WS_Z2N + (size_t)SEQ * FH * 2;
constexpr size_t WS_SP  = WS_WSP + (size_t)SEQ * SEQ * 2;
constexpr size_t WS_U   = WS_SP  + (size_t)2 * SEQ * DM * 4;
constexpr size_t WS_END = WS_U   + (size_t)SEQ * FH * 2;

DI unsigned rfl(unsigned x) { return __builtin_amdgcn_readfirstlane(x); }
DI unsigned cvtpk(float lo, float hi) { f32x2 v = {lo, hi}; bf16x2 b = __builtin_convertvector(v, bf16x2); return __builtin_bit_cast(unsigned, b); }
DI float bflo(unsigned w) { return __uint_as_float(w << 16); }
DI float bfhi(unsigned w) { return __uint_as_float(w & 0xffff0000u); }
DI rsrc_t mk_srd(const void* p, unsigned bytes) { return __builtin_amdgcn_make_buffer_rsrc((void*)p, (short)0, (int)bytes, SRD_WORD3); }
DI float max3f(float a, float b, float c) { float r; asm("v_max3_f32 %0, %1, %2, %3" : "=v"(r) : "v"(a), "v"(b), "v"(c)); return r; }
DI float max2f(float a, float b) { float r; asm("v_max_f32_e32 %0, %1, %2" : "=v"(r) : "v"(a), "v"(b)); return r; }
DI float fadd_s(float a, float b) { float r; asm("v_add_f32_e32 %0, %1, %2" : "=v"(r) : "v"(a), "v"(b)); return r; }
DI float fmul_s(float a, float b) { float r; asm("v_mul_f32_e32 %0, %1, %2" : "=v"(r) : "v"(a), "v"(b)); return r; }
DI float fsub_s(float a, float b) { float r; asm("v_sub_f32_e32 %0, %1, %2" : "=v"(r) : "v"(a), "v"(b)); return r; }
template <int N> DI void wait_vm_bar() { asm volatile("s_waitcnt vmcnt(%c0) lgkmcnt(0)\n\ts_barrier" :: "i"(N > 63 ? 63 : N) : "memory"); }
DI void dma16(unsigned m0v, unsigned voff, rsrc_t srd, unsigned soff) {
  asm volatile("s_mov_b32 m0, %0\n\ts_nop 0\n\tbuffer_load_dwordx4 %1, %2, %3 offen lds" :: "s"(m0v), "v"(voff), "s"(srd), "s"(soff) : "m0", "memory");
}
template <bool NATK, bool SWAPN> DI unsigned tr_lane(unsigned lane, unsigned halfstride) {
  const unsigned c = lane & 3u, cs = SWAPN ? (((c & 1u) << 1) | (c >> 1)) : c, hi = lane >> 5;
  return (cs << 3) | (((lane >> 2) & 3u) << 6) | (((lane >> 4) & 1u) << 5) | (NATK ? hi * halfstride : (hi << 8));
}

__global__ __launch_bounds__(256) void k_ln(const unsigned short* __restrict__ x, unsigned char* __restrict__ xnF) {
  const unsigned lane = threadIdx.x & 63u, wid = threadIdx.x >> 6, l = lane & 31u;
  const unsigned row = blockIdx.x * 8u + wid * 2u + (lane >> 5);
  const u32x4* src = (const u32x4*)(x + (size_t)row * DM);
  u32x4 v[3]; v[0] = src[l]; v[1] = src[l + 32]; v[2] = (l < 16u) ? src[l + 64] : (u32x4){0u, 0u, 0u, 0u};
  float f[24]; float s = 0.f;
#pragma unroll
  for (int i = 0; i < 3; ++i)
#pragma unroll
    for (int e = 0; e < 4; ++e) { f[8 * i + 2 * e] = bflo(v[i][e]); f[8 * i + 2 * e + 1] = bfhi(v[i][e]); s += f[8 * i + 2 * e] + f[8 * i + 2 * e + 1]; }
#pragma unroll
  for (int o = 16; o; o >>= 1) s += __shfl_xor(s, o);
  const float mean = s * (1.f / DM); float q = 0.f;
#pragma unroll
  for (int i = 0; i < 24; ++i) { f[i] -= mean; if (i < 16 || l < 16u) q += f[i] * f[i]; }
#pragma unroll
  for (int o = 16; o; o >>= 1) q += __shfl_xor(q, o);
  const float rstd = 1.f / sqrtf(q * (1.f / DM) + 1e-5f);
#pragma unroll
  for (int i = 0; i < 3; ++i) {
    const unsigned c = l + 32u * i;
    if (i < 2 || l < 16u) {
      u32x4 w;
#pragma unroll
      for (int e = 0; e < 4; ++e) w[e] = cvtpk(f[8 * i + 2 * e] * rstd, f[8 * i + 2 * e + 1] * rstd);
      *(u32x4*)(xnF + ((size_t)((row >> 5) * 40u + (c >> 1)) * 64u + (c & 1u) * 32u + (row & 31u)) * 16u) = w;
    }
  }
}

template <int NT, int KS> struct GemmC {
  static constexpr int P = NT * KS / 4, TILEB = NT * KS * 1024, G = P + KS;
  unsigned vw[P];
  unsigned vx[2];
  unsigned vtr;
  unsigned m0w;
  unsigned wstride;
  rsrc_t srdW, srdX;
  f32x16 acc[NT];
};
template <int NT, int KS> constexpr int GA(int p, int ks, int nt) { return p * (KS * NT * 4) + (ks * NT + nt) * 4; }
template <int NT, int KS> constexpr int GX(int u, int ks) { return 2 * KS * NT * 4 + (u * KS + ks) * 4; }

template <int NT, int KS, bool NATK, int PAR, int I> DI void g_tr(unsigned vaddr) {
  constexpr int f = I >> 1, half = I & 1, ks = f / NT, nt = f % NT, a = GA<NT, KS>(PAR, ks, nt) + 2 * half;
  constexpr int off = NATK ? ((2 * ks) * NT + nt) * 512 + 256 * half : ((2 * ks + half) * NT + nt) * 512;
  asm volatile("ds_read_b64_tr_b16 a[%c1:%c2], %0 offset:%c3" :: "v"(vaddr), "i"(a), "i"(a + 1), "i"(off) : "memory", ACLOB);
}
template <int NT, int KS, int XI, int KSI> DI void g_xload(GemmC<NT, KS>& c, unsigned soff) {
  constexpr int a = GX<NT, KS>(XI, KSI);
  asm volatile("buffer_load_dwordx4 a[%c3:%c4], %0, %1, %2 offen offset:%c5" :: "v"(c.vx[KSI >> 2]), "s"(c.srdX), "s"(soff), "i"(a), "i"(a + 3), "i"((KSI & 3) * 1024) : "memory", ACLOB);
}
template <int NT, int KS, int PAR, int XI, bool ZERO, int M> DI void g_mfma(GemmC<NT, KS>& c) {
  constexpr int ks = M / NT, nt = M % NT, a = GA<NT, KS>(PAR, ks, nt), b = GX<NT, KS>(XI, ks);
  if constexpr (ZERO && ks == 0) asm volatile("v_mfma_f32_32x32x16_bf16 %0, a[%c1:%c2], a[%c3:%c4], 0" : "=v"(c.acc[nt]) : "i"(a), "i"(a + 3), "i"(b), "i"(b + 3) : ACLOB);
  else asm volatile("v_mfma_f32_32x32x16_bf16 %0, a[%c1:%c2], a[%c3:%c4], %0" : "+v"(c.acc[nt]) : "i"(a), "i"(a + 3), "i"(b), "i"(b + 3) : ACLOB);
}
template <int NT, int KS, int... Q> DI void g_wtile(GemmC<NT, KS>& c, unsigned slot, unsigned soff, iseq<Q...>) {
  (dma16(c.m0w + slot * GemmC<NT, KS>::TILEB + 1024u * Q, c.vw[Q], c.srdW, soff), ...);
}
template <int NT, int KS, int XI, int... Q> DI void g_xtile(GemmC<NT, KS>& c, unsigned soff, iseq<Q...>) { (g_xload<NT, KS, XI, Q>(c, soff), ...); }
template <int NT, int KS, bool NATK, int PAR, int... I> DI void g_trall(unsigned vaddr, iseq<I...>) { (g_tr<NT, KS, NATK, PAR, I>(vaddr), ...); }

template <int NT, int KS, bool NATK, int T4, bool ZERO, bool LW, bool LX, bool RD, int M> DI void g_gap(GemmC<NT, KS>& c, unsigned vrd, unsigned sw, unsigned sx) {
  constexpr int NM = NT * KS, P = GemmC<NT, KS>::P;
  g_mfma<NT, KS, (T4 & 1), T4, ZERO, M>(c);
  if constexpr (RD) {
    if constexpr (3 * M + 0 < 2 * NM) g_tr<NT, KS, NATK, ((T4 + 1) & 1), 3 * M + 0>(vrd);
    if constexpr (3 * M + 1 < 2 * NM) g_tr<NT, KS, NATK, ((T4 + 1) & 1), 3 * M + 1>(vrd);
    if constexpr (3 * M + 2 < 2 * NM) g_tr<NT, KS, NATK, ((T4 + 1) & 1), 3 * M + 2>(vrd);
  }
  constexpr int j = M - (NM - P - KS - 1);
  if constexpr (LW && j >= 0 && j < P) dma16(c.m0w + T4 * GemmC<NT, KS>::TILEB + 1024u * j, c.vw[j], c.srdW, sw);
  if constexpr (LX && j >= P && j < P + KS) g_xload<NT, KS, ((T4 + 3) & 3), j - P>(c, sx);
  GAP();
}
template <int NT, int KS, bool NATK, int T4, bool ZERO, bool LW, bool LX, bool RD, int VM, int... Ms> DI void g_step_(GemmC<NT, KS>& c, int t, iseq<Ms...>) {
  const unsigned vrd = c.vtr + ((T4 + 1) & 3) * GemmC<NT, KS>::TILEB;
  const unsigned sw = (unsigned)(t + 4) * c.wstride, sx = (unsigned)(t + 3) * (KS * 1024u);
  wait_vm_bar<VM>();
  GAP();
  (g_gap<NT, KS, NATK, T4, ZERO, LW, LX, RD, Ms>(c, vrd, sw, sx), ...);
}
template <int NT, int KS, int NTILES> constexpr int g_vm(int t) {
  int n = 0;
  for (int j = t - 2; j <= t - 1; ++j) { if (j + 4 < NTILES) n += NT * KS / 4; if (j + 3 < NTILES) n += KS; }
  return n;
}
template <int NT, int KS, bool NATK, int NTILES, int T> DI void g_step_static(GemmC<NT, KS>& c) {
  g_step_<NT, KS, NATK, (T & 3), (T == 0), (T + 4 < NTILES), (T + 3 < NTILES), (T + 1 < NTILES), g_vm<NT, KS, NTILES>(T)>(c, T, make_iseq<NT * KS>{});
}
template <int NT, int KS, bool NATK, int NTILES, int T0, int... Ts> DI void g_tail(GemmC<NT, KS>& c, iseq<Ts...>) { (g_step_static<NT, KS, NATK, NTILES, T0 + Ts>(c), ...); }
template <int NT, int KS, bool NATK, int NTILES> DI void gemm_run(GemmC<NT, KS>& c) {
  constexpr int P = GemmC<NT, KS>::P, G = GemmC<NT, KS>::G;
  asm volatile("s_nop 4" ::: "memory");
  g_wtile<NT, KS>(c, 0u, 0u, make_iseq<P>{});
  g_wtile<NT, KS>(c, 1u, c.wstride, make_iseq<P>{});      g_xtile<NT, KS, 0>(c, 0u, make_iseq<KS>{});
  g_wtile<NT, KS>(c, 2u, 2u * c.wstride, make_iseq<P>{}); g_xtile<NT, KS, 1>(c, KS * 1024u, make_iseq<KS>{});
  g_wtile<NT, KS>(c, 3u, 3u * c.wstride, make_iseq<P>{}); g_xtile<NT, KS, 2>(c, 2u * KS * 1024u, make_iseq<KS>{});
  wait_vm_bar<3 * G>();
  g_trall<NT, KS, NATK, 0>(c.vtr, make_iseq<2 * NT * KS>{});
  g_tail<NT, KS, NATK, NTILES, 0>(c, make_iseq<4>{});
  constexpr int T0 = 4 * ((NTILES - 4) / 4);
  for (int t = 4; t < T0; t += 4) {
    g_step_<NT, KS, NATK, 0, false, true, true, true, 2 * G>(c, t, make_iseq<NT * KS>{});
    g_step_<NT, KS, NATK, 1, false, true, true, true, 2 * G>(c, t + 1, make_iseq<NT * KS>{});
    g_step_<NT, KS, NATK, 2, false, true, true, true, 2 * G>(c, t + 2, make_iseq<NT * KS>{});
    g_step_<NT, KS, NATK, 3, false, true, true, true, 2 * G>(c, t + 3, make_iseq<NT * KS>{});
  }
  g_tail<NT, KS, NATK, NTILES, T0>(c, make_iseq<NTILES - T0>{});
  asm volatile("s_nop 7\n\ts_nop 7" ::: "memory");
#pragma unroll
  for (int nt = 0; nt < NT; ++nt) asm volatile("" : "+v"(c.acc[nt]));
  GAP();
}
template <int NT, int KS, bool NATK, bool SWAPN> DI void gemm_setup_rm(GemmC<NT, KS>& c, unsigned ldsb, unsigned lane, unsigned wid, unsigned ldB) {
  constexpr int P = GemmC<NT, KS>::P;
#pragma unroll
  for (int q = 0; q < P; ++q) { const unsigned p = wid * P + q, kg = p / (NT / 2), cp = p % (NT / 2);
    c.vw[q] = ldB * (8u * kg + ((lane >> 2) & 7u)) + 128u * cp + 64u * (lane >> 5) + 16u * (lane & 3u); }
  c.wstride = ldB * 16u * KS;
  c.m0w = rfl(ldsb + 1024u * P * wid);
  c.vtr = ldsb + tr_lane<NATK, SWAPN>(lane, NT * 512u);
}

DI float gelu_tanh(float v) {
  const float y2 = v * (-2.3022081985f - 0.1029432397f * v * v);
  return v * __builtin_amdgcn_rcpf(1.f + __builtin_amdgcn_exp2f(y2));
}
__global__ __launch_bounds__(256, 1) void k_zgemm(const unsigned char* __restrict__ xnF, const unsigned short* __restrict__ w_in, unsigned char* __restrict__ zF) {
  extern __shared__ __attribute__((aligned(16))) char lds[];
  const unsigned lane = threadIdx.x & 63u, wid = rfl(threadIdx.x >> 6);
  const unsigned xcd = blockIdx.x & 7u, idx = blockIdx.x >> 3;
  const unsigned rb128 = 4u * (xcd >> 1) + idx / 7u, cb = 7u * (xcd & 1u) + idx % 7u, rb = 4u * rb128 + wid;
  GemmC<4, 5> c;
  gemm_setup_rm<4, 5, true, false>(c, (unsigned)(size_t)lds, lane, wid, FF * 2u);
  c.srdW = mk_srd((const char*)w_in + cb * 256u, DM * FF * 2u - cb * 256u);
  c.srdX = mk_srd(xnF, SEQ * DM * 2u);
  c.vx[0] = rb * 40u * 1024u + lane * 16u; c.vx[1] = c.vx[0] + 4096u;
  gemm_run<4, 5, true, 8>(c);
#pragma unroll
  for (int nt = 0; nt < 4; ++nt) {
    const unsigned ct = 4u * cb + nt;
    if (ct < 54u) {
#pragma unroll
      for (int a = 0; a < 2; ++a) {
        u32x4 w;
#pragma unroll
        for (int e = 0; e < 4; ++e) w[e] = cvtpk(gelu_tanh(c.acc[nt][8 * a + 2 * e]), gelu_tanh(c.acc[nt][8 * a + 2 * e + 1]));
        *(u32x4*)(zF + ((size_t)(rb * 108u + 2u * ct + a) * 64u + lane) * 16u) = w;
      }
    }
  }
}

template <int NT> DI void spatial_unit(char* lds, unsigned lane, unsigned wid, unsigned rb, unsigned ct0, const unsigned char* wspF, const unsigned char* z2nT,
                                       const unsigned char* zF, const unsigned short* b_sp, unsigned char* uF) {
  GemmC<NT, 4> c;
  const unsigned ldsb = (unsigned)(size_t)lds;
#pragma unroll
  for (int q = 0; q < NT; ++q) { const unsigned sg = 2u * (wid * NT + q) + (lane >> 5), kg = sg / NT, nt = sg % NT;
    c.vw[q] = (kg * 27u + ct0 + nt) * 512u + (lane & 31u) * 16u; }
  c.wstride = 8u * 27u * 512u;
  c.m0w = rfl(ldsb + 1024u * NT * wid);
  c.vtr = ldsb + tr_lane<true, true>(lane, NT * 512u);
  c.srdW = mk_srd(z2nT, SEQ * FH * 2u);
  c.srdX = mk_srd(wspF, SEQ * SEQ * 2u);
  c.vx[0] = rb * 128u * 1024u + lane * 16u; c.vx[1] = c.vx[0];
  gemm_run<NT, 4, true, 32>(c);
  const float bias = bflo((unsigned)b_sp[rb * 32u + (lane & 31u)]);
#pragma unroll
  for (int nt = 0; nt < NT; ++nt)
#pragma unroll
    for (int a = 0; a < 2; ++a) {
      const unsigned ks = 2u * (ct0 + nt) + a;
      const u32x4 z1 = *(const u32x4*)(zF + ((size_t)(rb * 108u + ks) * 64u + lane) * 16u);
      u32x4 w;
#pragma unroll
      for (int e = 0; e < 4; ++e) w[e] = cvtpk(bflo(z1[e]) * (c.acc[nt][8 * a + 2 * e] + bias), bfhi(z1[e]) * (c.acc[nt][8 * a + 2 * e + 1] + bias));
      *(u32x4*)(uF + ((size_t)(rb * 54u + ks) * 64u + lane) * 16u) = w;
    }
}
__global__ __launch_bounds__(256, 1) void k_mid(const unsigned char* __restrict__ OF, const unsigned short* __restrict__ wo, unsigned char* __restrict__ sP,
                                                const unsigned char* __restrict__ wspF, const unsigned char* __restrict__ z2nT, const unsigned char* __restrict__ zF,
                                                const unsigned short* __restrict__ b_sp, unsigned char* __restrict__ uF) {
  extern __shared__ __attribute__((aligned(16))) char lds[];
  const unsigned lane = threadIdx.x & 63u, wid = rfl(threadIdx.x >> 6);
  const unsigned xcd = blockIdx.x & 7u, idx = blockIdx.x >> 3;
  if (idx >= 12u) {
    const unsigned i = idx - 12u, kh = xcd & 1u, rb128 = 4u * (xcd >> 1) + i / 5u, cb = i % 5u, rb = 4u * rb128 + wid;
    GemmC<4, 4> c;
    gemm_setup_rm<4, 4, false, true>(c, (unsigned)(size_t)lds, lane, wid, DM * 2u);
    const unsigned wofs = kh * 2048u * DM * 2u + cb * 256u;
    c.srdW = mk_srd((const char*)wo + wofs, NH * DH * DM * 2u - wofs);
    c.srdX = mk_srd(OF, SEQ * NH * DH * 2u);
    c.vx[0] = (rb * 256u + kh * 128u) * 1024u + lane * 16u; c.vx[1] = c.vx[0];
    gemm_run<4, 4, false, 32>(c);
#pragma unroll
    for (int nt = 0; nt < 4; ++nt)
#pragma unroll
      for (int g = 0; g < 4; ++g) {
        f32x4 w = {c.acc[nt][4 * g], c.acc[nt][4 * g + 1], c.acc[nt][4 * g + 2], c.acc[nt][4 * g + 3]};
        *(f32x4*)(sP + ((size_t)(((kh * 64u + rb) * 20u + 4u * cb + nt) * 4u + g) * 64u + lane) * 16u) = w;
      }
  } else {
    const unsigned rb128 = 4u * (xcd >> 1) + idx / 3u, j = 3u * (xcd & 1u) + idx % 3u, rb = 4u * rb128 + wid;
    if (j & 1u) spatial_unit<4>(lds, lane, wid, rb, 15u + 4u * (j >> 1), wspF, z2nT, zF, b_sp, uF);
    else        spatial_unit<5>(lds, lane, wid, rb, 5u * (j >> 1), wspF, z2nT, zF, b_sp, uF);
  }
}

__global__ __launch_bounds__(256, 1) void k_out(const unsigned char* __restrict__ uF, const unsigned short* __restrict__ w_out, const unsigned char* __restrict__ sP,
                                                const unsigned short* __restrict__ x, unsigned short* __restrict__ out) {
  extern __shared__ __attribute__((aligned(16))) char lds[];
  const unsigned lane = threadIdx.x & 63u, wid = rfl(threadIdx.x >> 6);
  const unsigned xcd = blockIdx.x & 7u, idx = blockIdx.x >> 3;
  const unsigned rb128 = 4u * (xcd >> 1) + idx / 5u, cb = 5u * (xcd & 1u) + idx % 5u, rb = 4u * rb128 + wid;
  GemmC<2, 6> c;
  gemm_setup_rm<2, 6, false, true>(c, (unsigned)(size_t)lds, lane, wid, DM * 2u);
  c.srdW = mk_srd((const char*)w_out + cb * 128u, FH * DM * 2u - cb * 128u);
  c.srdX = mk_srd(uF, SEQ * FH * 2u);
  c.vx[0] = rb * 54u * 1024u + lane * 16u; c.vx[1] = c.vx[0] + 4096u;
  gemm_run<2, 6, false, 9>(c);
  const unsigned row = rb * 32u + (lane & 31u), hi = lane >> 5;
#pragma unroll
  for (int nt = 0; nt < 2; ++nt) {
    const unsigned ct = 2u * cb + nt;
#pragma unroll
    for (int a = 0; a < 2; ++a) {
      const size_t eo = (size_t)row * DM + 32u * ct + 16u * a + 8u * hi;
      const u32x4 xv = *(const u32x4*)(x + eo);
      float sv[8];
#pragma unroll
      for (int g = 0; g < 2; ++g) {
        const f32x4 p0 = *(const f32x4*)(sP + ((size_t)((rb * 20u + ct) * 4u + 2 * a + g) * 64u + lane) * 16u);
        const f32x4 p1 = *(const f32x4*)(sP + ((size_t)(((64u + rb) * 20u + ct) * 4u + 2 * a + g) * 64u + lane) * 16u);
#pragma unroll
        for (int e = 0; e < 4; ++e) sv[4 * g + e] = p0[e] + p1[e];
      }
      u32x4 w;
#pragma unroll
      for (int e = 0; e < 4; ++e) {
        const float b0 = c.acc[nt][8 * a + 2 * e], b1 = c.acc[nt][8 * a + 2 * e + 1];
        const float g0 = b0 * __builtin_amdgcn_rcpf(1.f + __builtin_amdgcn_exp2f(-1.4426950408889634f * b0));
        const float g1 = b1 * __builtin_amdgcn_rcpf(1.f + __builtin_amdgcn_exp2f(-1.4426950408889634f * b1));
        w[e] = cvtpk(bflo(xv[e]) + sv[2 * e] * g0, bfhi(xv[e]) + sv[2 * e + 1] * g1);
      }
      *(u32x4*)(out + eo) = w;
    }
  }
}

constexpr int NTILE = SEQ / 64;
constexpr unsigned KRS = 256u, KTILEB = 64u * KRS;
constexpr float THR = 8.0f;
#define KRING 0x0000u
#define VRING 0x8000u
#define WRING 0x10000u
DI float w4_aone() { unsigned l; asm volatile("v_mbcnt_lo_u32_b32 %0, -1, 0\n\tv_mbcnt_hi_u32_b32 %0, -1, %0" : "=v"(l)); return l < 32u ? 1.f : 0.f; }
DI void negm_bcast(f32x16& negm, float aone, float neg_mhat) { asm volatile("s_nop 1\n\tv_mfma_f32_32x32x2_f32 %0, %1, %2, 0" : "+v"(negm) : "v"(aone), "v"(neg_mhat)); }
struct W4C {
  unsigned vkb[4], vvb, vksrc[2], vvsrc, m0k, m0v, sofk, sofv, ldsb;
  rsrc_t srdK, srdV;
  f32x16 S[2][2][2];
  f32x16 NEGM[2];
  bf16x8 Vf[4][4];
  bf16x8 Pk[2][4];
  bf16x4 vlo[4][4], vup[4][4];
  float mhat[2], lsum[2], fct[2]; bool pend;
  float rs0[2], rs1[2], pmx0[2], pmx1[2], rmax[2], delta[2];
  unsigned pm0[8], psf[8];
};
constexpr int AO_(int db, int qb) { return 16 * (2 * db + qb); }
constexpr int AQ_(int qb, int d0) { return 128 + 32 * qb + 4 * d0; }
constexpr int AK_(int kb, int d0) { return 192 + 32 * kb + 4 * d0; }
template <int I> DI void tr_read(W4C& c, unsigned vslot) {
  constexpr int ks = I >> 3, db = (I >> 1) & 3;
  if constexpr ((I & 1) == 0) asm volatile("ds_read_b64_tr_b16 %0, %1 offset:%2" : "=v"(c.vlo[db][ks]) : "v"(c.vvb + vslot), "i"(4096 * ks + 512 * db) : "memory");
  else                        asm volatile("ds_read_b64_tr_b16 %0, %1 offset:%2" : "=v"(c.vup[db][ks]) : "v"(c.vvb + vslot), "i"(4096 * ks + 2048 + 512 * db) : "memory");
}
template <int I> DI void k_read(W4C& c, unsigned slot) {
  constexpr int kb = I >> 3, d0 = I & 7;
  asm volatile("ds_read_b128 a[%c1:%c2], %0 offset:%c3" :: "v"(c.vkb[d0 & 3] + slot), "i"(AK_(kb, d0)), "i"(AK_(kb, d0) + 3), "i"(8192 * kb + 128 * (d0 >> 2)) : "memory", ACLOB_HI);
}
template <int X, bool FIRST, int M> DI void qk_mfma(W4C& c) {
  constexpr int d0 = M >> 2, kb = (M >> 1) & 1, qb = M & 1, ak = AK_(kb, d0), aq = AQ_(qb, d0);
  if constexpr (d0 == 0) {
    if constexpr (FIRST) asm volatile("v_mfma_f32_32x32x16_bf16 %0, a[%c1:%c2], a[%c3:%c4], 0" : "=&v"(c.S[X][kb][qb]) : "i"(ak), "i"(ak + 3), "i"(aq), "i"(aq + 3) : ACLOB);
    else                 asm volatile("v_mfma_f32_32x32x16_bf16 %0, a[%c2:%c3], a[%c4:%c5], %1" : "=&v"(c.S[X][kb][qb]) : "v"(c.NEGM[qb]), "i"(ak), "i"(ak + 3), "i"(aq), "i"(aq + 3) : ACLOB);
  } else asm volatile("v_mfma_f32_32x32x16_bf16 %0, a[%c1:%c2], a[%c3:%c4], %0" : "+v"(c.S[X][kb][qb]) : "i"(ak), "i"(ak + 3), "i"(aq), "i"(aq + 3) : ACLOB);
}
template <int M, bool ZERO> DI void pv_mfma(W4C& c) {
  constexpr int ks = M >> 3, db = (M >> 1) & 3, qb = M & 1, ao = AO_(db, qb);
  if constexpr (ZERO && ks == 0) asm volatile("v_mfma_f32_32x32x16_bf16 a[%c2:%c3], %0, %1, 0" :: "v"(c.Vf[db][ks]), "v"(c.Pk[qb][ks]), "i"(ao), "i"(ao + 15) : ACLOB_LO);
  else                         asm volatile("v_mfma_f32_32x32x16_bf16 a[%c2:%c3], %0, %1, a[%c2:%c3]" :: "v"(c.Vf[db][ks]), "v"(c.Pk[qb][ks]), "i"(ao), "i"(ao + 15) : ACLOB_LO);
}
template <int X, int QB, int KS, int P> DI void pack1(W4C& c) {
  u32x4 w = __builtin_bit_cast(u32x4, c.Pk[QB][KS]);
  w[P] = cvtpk(c.S[X][KS >> 1][QB][8 * (KS & 1) + 2 * P], c.S[X][KS >> 1][QB][8 * (KS & 1) + 2 * P + 1]);
  c.Pk[QB][KS] = __builtin_bit_cast(bf16x8, w);
}
template <int X, int J> DI void rowsum1(W4C& c) {
  constexpr int qb = J >> 4, ks = (J >> 2) & 3, p = J & 3;
  c.rs0[qb] = fadd_s(c.rs0[qb], c.S[X][ks >> 1][qb][8 * (ks & 1) + 2 * p]); c.rs1[qb] = fadd_s(c.rs1[qb], c.S[X][ks >> 1][qb][8 * (ks & 1) + 2 * p + 1]);
}
template <int X, bool FIRST, int M> DI void gapA(W4C& c, unsigned vslot, const int t) {
  constexpr int Xp = X ^ 1;
  qk_mfma<X, FIRST, M>(c);
  if constexpr (M >= 16 && M <= 31) {
    constexpr int q = (M - 16) >> 1;
    if constexpr (((M - 16) & 1) == 0) {
      if constexpr (q < 4) c.pm0[q] = c.m0k + (unsigned)((t + 2) & 1) * 0x4000u + 1024u * q; else c.pm0[q] = c.m0v + (unsigned)((t + 1) & 1) * 0x4000u + 1024u * (q - 4);
      asm volatile("" : "+s"(c.pm0[q]));
    } else {
      if constexpr (q < 4) c.psf[q] = KTILEB * (unsigned)(t + 2) + c.sofk + KRS * 4u * q; else c.psf[q] = KTILEB * (unsigned)(t + 1) + c.sofv + KRS * 8u * ((q - 4) >> 1) + 128u * ((q - 4) & 1);
      asm volatile("" : "+s"(c.psf[q]));
    }
  }
  if constexpr (M == 31) asm volatile("s_nop 4" : "+s"(c.pm0[0]), "+s"(c.pm0[1]), "+s"(c.pm0[2]), "+s"(c.pm0[3]), "+s"(c.pm0[4]), "+s"(c.pm0[5]), "+s"(c.pm0[6]), "+s"(c.pm0[7]),
                                                  "+s"(c.psf[0]), "+s"(c.psf[1]), "+s"(c.psf[2]), "+s"(c.psf[3]), "+s"(c.psf[4]), "+s"(c.psf[5]), "+s"(c.psf[6]), "+s"(c.psf[7]));
  if constexpr (!FIRST && M == 8) asm volatile("" :: "v"(c.NEGM[0]), "v"(c.NEGM[1]));
  if constexpr (!FIRST) {
    constexpr int base = M + (M >= 4) + (M >= 8) + (M >= 12) + (M >= 16) + (M >= 20) + (M >= 24);
    if constexpr (M <= 25) tr_read<base>(c, vslot);
    if constexpr (M == 3 || M == 7 || M == 11 || M == 15 || M == 19 || M == 23) tr_read<base + 1>(c, vslot);
    if constexpr (M < 16) {
      constexpr int e0 = 2 * M, e1 = 2 * M + 1;
      c.S[Xp][1][e0 >> 4][e0 & 15] = __builtin_amdgcn_exp2f(c.S[Xp][1][e0 >> 4][e0 & 15]);
      c.S[Xp][1][e1 >> 4][e1 & 15] = __builtin_amdgcn_exp2f(c.S[Xp][1][e1 >> 4][e1 & 15]);
      pack1<Xp, (M >> 3), ((M >> 2) & 1), (M & 3)>(c);
    } else {
      constexpr int cc = M - 16;
      pack1<Xp, (cc >> 3), 2 + ((cc >> 2) & 1), (cc & 3)>(c);
      rowsum1<Xp, 16 * (cc >> 3) + 8 + 4 * ((cc >> 2) & 1) + (cc & 3)>(c);
    }
  }
  GAP();
}
#define W4_VLO "+v"(c.vlo[0][0]), "+v"(c.vlo[0][1]), "+v"(c.vlo[0][2]), "+v"(c.vlo[0][3]), "+v"(c.vlo[1][0]), "+v"(c.vlo[1][1]), "+v"(c.vlo[1][2]), "+v"(c.vlo[1][3]), \
               "+v"(c.vlo[2][0]), "+v"(c.vlo[2][1]), "+v"(c.vlo[2][2]), "+v"(c.vlo[2][3]), "+v"(c.vlo[3][0]), "+v"(c.vlo[3][1]), "+v"(c.vlo[3][2]), "+v"(c.vlo[3][3])
#define W4_VUP "+v"(c.vup[0][0]), "+v"(c.vup[0][1]), "+v"(c.vup[0][2]), "+v"(c.vup[0][3]), "+v"(c.vup[1][0]), "+v"(c.vup[1][1]), "+v"(c.vup[1][2]), "+v"(c.vup[1][3]), \
               "+v"(c.vup[2][0]), "+v"(c.vup[2][1]), "+v"(c.vup[2][2]), "+v"(c.vup[2][3]), "+v"(c.vup[3][0]), "+v"(c.vup[3][1]), "+v"(c.vup[3][2]), "+v"(c.vup[3][3])
DI void v_concat(W4C& c) {
#pragma unroll
  for (int ks = 0; ks < 4; ++ks)
#pragma unroll
    for (int db = 0; db < 4; ++db) c.Vf[db][ks] = __builtin_shufflevector(c.vlo[db][ks], c.vup[db][ks], 0, 1, 2, 3, 4, 5, 6, 7);
}
DI void lsum_merge(W4C& c) {
#pragma unroll
  for (int qb = 0; qb < 2; ++qb) {
    const float rs = fadd_s(c.rs0[qb], c.rs1[qb]);
    auto pr = __builtin_amdgcn_permlane32_swap(__float_as_uint(rs), __float_as_uint(rs), false, false);
    c.lsum[qb] = fadd_s(c.lsum[qb], fadd_s(__uint_as_float(pr[0]), __uint_as_float(pr[1])));
  }
}
template <int X, bool FIRST, int... Ms> DI void phaseA_(W4C& c, const int t, iseq<Ms...>) {
  const unsigned vslot = (unsigned)((t - 1) & 1) * 0x4000u;
  (gapA<X, FIRST, Ms>(c, vslot, t), ...);
  if constexpr (!FIRST) lsum_merge(c);
  if constexpr (FIRST) asm volatile("s_waitcnt vmcnt(0) lgkmcnt(0)\n\ts_barrier" ::: "memory");
  else { asm volatile("s_waitcnt vmcnt(0) lgkmcnt(0)" : W4_VLO :: "memory"); asm volatile("s_barrier" : W4_VUP :: "memory"); }
  GAP();
  if constexpr (!FIRST) v_concat(c);
}
template <int X, bool FIRST> DI void phaseA(W4C& c, const int t) { phaseA_<X, FIRST>(c, t, make_iseq<32>{}); }

template <int X, bool PEEL, bool ZERO1, bool DK, bool DV, bool KR, int M> DI void gapB(W4C& c, const unsigned kslot_rd) {
  if constexpr (!PEEL) pv_mfma<M, ZERO1>(c);
  if constexpr (M < 4 && DK) dma16(c.pm0[M], c.vksrc[M & 1], c.srdK, c.psf[M]);
  else if constexpr (M >= 4 && M < 8 && DV) dma16(c.pm0[M], c.vvsrc, c.srdV, c.psf[M]);
  if constexpr (M >= 1 && M <= 16 && KR) k_read<M - 1>(c, kslot_rd);
  if constexpr (M >= 8 && M < 16) {
    constexpr int i = M - 8, q = i >> 2, r = 4 * (i & 3);
    if constexpr ((i & 3) == 0) { c.pmx0[q] = max3f(c.S[X][0][q][0], c.S[X][0][q][1], c.S[X][1][q][0]); c.pmx1[q] = max3f(c.S[X][0][q][2], c.S[X][0][q][3], c.S[X][1][q][1]);
                                  c.pmx0[q] = max3f(c.pmx0[q], c.S[X][1][q][2], c.S[X][1][q][3]); }
    else                        { c.pmx0[q] = max3f(c.pmx0[q], c.S[X][0][q][r], c.S[X][0][q][r + 1]); c.pmx1[q] = max3f(c.pmx1[q], c.S[X][0][q][r + 2], c.S[X][0][q][r + 3]);
                                  c.pmx0[q] = max3f(c.pmx0[q], c.S[X][1][q][r], c.S[X][1][q][r + 1]); c.pmx1[q] = max3f(c.pmx1[q], c.S[X][1][q][r + 2], c.S[X][1][q][r + 3]); }
  }
  if constexpr (M == 16 || M == 17) {
    constexpr int q = M - 16; const float mm = max2f(c.pmx0[q], c.pmx1[q]);
    auto pr = __builtin_amdgcn_permlane32_swap(__float_as_uint(mm), __float_as_uint(mm), false, false);
    c.rmax[q] = max2f(__uint_as_float(pr[0]), __uint_as_float(pr[1]));
  }
  if constexpr (M == 18) {
    if constexpr (PEEL) {
#pragma unroll
      for (int q = 0; q < 2; ++q) { c.mhat[q] = c.rmax[q]; c.lsum[q] = 0.f; c.fct[q] = 1.f;
#pragma unroll
        for (int kb = 0; kb < 2; ++kb) { _Pragma("unroll") for (int e = 0; e < 16; ++e) c.S[X][kb][q][e] = fsub_s(c.S[X][kb][q][e], c.rmax[q]); }
        negm_bcast(c.NEGM[q], w4_aone(), -c.mhat[q]);
      }
    } else {
      const bool grew = (c.rmax[0] > THR) | (c.rmax[1] > THR);
      if (__builtin_expect(__any((int)grew), 0)) {
#pragma unroll
        for (int q = 0; q < 2; ++q) { const float dl = max2f(c.rmax[q], 0.f); c.delta[q] = dl; c.mhat[q] = fadd_s(c.mhat[q], dl); c.fct[q] = __builtin_amdgcn_exp2f(-dl);
#pragma unroll
          for (int kb = 0; kb < 2; ++kb) { _Pragma("unroll") for (int e = 0; e < 16; ++e) c.S[X][kb][q][e] = fsub_s(c.S[X][kb][q][e], dl); }
          negm_bcast(c.NEGM[q], w4_aone(), -c.mhat[q]);
        }
        c.pend = true;
      }
    }
  }
  if constexpr (M == 18) { c.rs0[0] = c.rs0[1] = c.rs1[0] = c.rs1[1] = 0.f; }
  if constexpr (M >= 19) {
    constexpr int g = M - 19, e_lo = (32 * g) / 13, e_hi = (32 * (g + 1)) / 13;
    {
      constexpr int p_lo = g ? (32 * (g - 1)) / 13 : 0, p_hi = g ? (32 * g) / 13 : 0;
      if constexpr (p_lo + 0 < p_hi) { constexpr int e = p_lo + 0; if constexpr (e & 1) c.rs1[e >> 4] = fadd_s(c.rs1[e >> 4], c.S[X][0][e >> 4][e & 15]); else c.rs0[e >> 4] = fadd_s(c.rs0[e >> 4], c.S[X][0][e >> 4][e & 15]); }
      if constexpr (p_lo + 1 < p_hi) { constexpr int e = p_lo + 1; if constexpr (e & 1) c.rs1[e >> 4] = fadd_s(c.rs1[e >> 4], c.S[X][0][e >> 4][e & 15]); else c.rs0[e >> 4] = fadd_s(c.rs0[e >> 4], c.S[X][0][e >> 4][e & 15]); }
      if constexpr (p_lo + 2 < p_hi) { constexpr int e = p_lo + 2; if constexpr (e & 1) c.rs1[e >> 4] = fadd_s(c.rs1[e >> 4], c.S[X][0][e >> 4][e & 15]); else c.rs0[e >> 4] = fadd_s(c.rs0[e >> 4], c.S[X][0][e >> 4][e & 15]); }
    }
    if constexpr (e_lo + 0 < e_hi) c.S[X][0][(e_lo + 0) >> 4][(e_lo + 0) & 15] = __builtin_amdgcn_exp2f(c.S[X][0][(e_lo + 0) >> 4][(e_lo + 0) & 15]);
    if constexpr (e_lo + 1 < e_hi) c.S[X][0][(e_lo + 1) >> 4][(e_lo + 1) & 15] = __builtin_amdgcn_exp2f(c.S[X][0][(e_lo + 1) >> 4][(e_lo + 1) & 15]);
    if constexpr (e_lo + 2 < e_hi) c.S[X][0][(e_lo + 2) >> 4][(e_lo + 2) & 15] = __builtin_amdgcn_exp2f(c.S[X][0][(e_lo + 2) >> 4][(e_lo + 2) & 15]);
  }
  if constexpr (M == 31) asm volatile("" :: "v"(c.S[X][0][0]), "v"(c.S[X][0][1]), "v"(c.NEGM[0]), "v"(c.NEGM[1]), "v"(c.rs0[0]), "v"(c.rs0[1]), "v"(c.rs1[0]), "v"(c.rs1[1]));
  GAP();
}
template <int R> DI void o_scale1(float f0, float f1) {
  float t; asm volatile("v_accvgpr_read_b32 %0, a[%c2]\n\tv_mul_f32 %0, %1, %0\n\tv_accvgpr_write_b32 a[%c2], %0" : "=&v"(t) : "v"(((R >> 4) & 1) ? f1 : f0), "i"(R) : ACLOB_LO);
}
template <int... Rs> DI void o_scale(float f0, float f1, iseq<Rs...>) { (o_scale1<Rs>(f0, f1), ...); }
template <int X, bool PEEL, bool ZERO1, bool DK, bool DV, bool KR, int... Ms> DI void phaseB_(W4C& c, const int t, iseq<Ms...>) {
  const unsigned kslot_rd = (unsigned)((t + 1) & 1) * 0x4000u;
  if constexpr (!PEEL) asm volatile("s_nop 1" ::: "memory");
  GAP();
  (gapB<X, PEEL, ZERO1, DK, DV, KR, Ms>(c, kslot_rd), ...);
#pragma unroll
  for (int e = (32 * 12) / 13; e < 32; ++e) { if (e & 1) c.rs1[e >> 4] = fadd_s(c.rs1[e >> 4], c.S[X][0][e >> 4][e & 15]); else c.rs0[e >> 4] = fadd_s(c.rs0[e >> 4], c.S[X][0][e >> 4][e & 15]); }
  if constexpr (!PEEL) {
    if (__builtin_expect(c.pend, 0)) {
      asm volatile("s_nop 7\n\ts_nop 7" ::: "memory");
      o_scale(c.fct[0], c.fct[1], make_iseq<128>{});
      asm volatile("s_nop 1" ::: "memory");
      c.lsum[0] = fmul_s(c.lsum[0], c.fct[0]); c.lsum[1] = fmul_s(c.lsum[1], c.fct[1]); c.pend = false;
    }
  }
  asm volatile("s_waitcnt lgkmcnt(0)" ::: "memory");
  GAP();
}
template <int X, bool PEEL, bool ZERO1, bool DK = true, bool DV = true, bool KR = true> DI void phaseB(W4C& c, const int t) { phaseB_<X, PEEL, ZERO1, DK, DV, KR>(c, t, make_iseq<32>{}); }
template <int... Is> DI void tr_reads(W4C& c, unsigned vslot, iseq<Is...>) { (tr_read<Is>(c, vslot), ...); }
template <int X, int... Cs> DI void packs(W4C& c, iseq<Cs...>) { (pack1<X, ((Cs >> 3) & 1), 2 * (Cs >> 4) + ((Cs >> 2) & 1), (Cs & 3)>(c), ...); }
template <int X, int J> DI void rowsum1_padded(W4C& c) { rowsum1<X, J>(c); asm volatile("s_nop 1"); }
template <int X, int... Js> DI void rowsums(W4C& c, iseq<Js...>) { (rowsum1_padded<X, Js>(c), ...); }
template <int... Ms> DI void pv_all(W4C& c, iseq<Ms...>) { (pv_mfma<Ms, false>(c), ...); }

struct QPC { unsigned vtr, vwsrc, m0w, sofw, vx; rsrc_t srdW, srdX; bf16x4 wl[2][4][4], wu[2][4][4]; };
template <int PAR, int I> DI void qp_tr(QPC& q, unsigned vaddr) {
  constexpr int ks = I >> 3, db = (I >> 1) & 3;
  if constexpr ((I & 1) == 0) asm volatile("ds_read_b64_tr_b16 %0, %1 offset:%2" : "=v"(q.wl[PAR][db][ks]) : "v"(vaddr), "i"(4096 * ks + 512 * db) : "memory");
  else                        asm volatile("ds_read_b64_tr_b16 %0, %1 offset:%2" : "=v"(q.wu[PAR][db][ks]) : "v"(vaddr), "i"(4096 * ks + 256 + 512 * db) : "memory");
}
template <int T, int Q> DI void qp_w(QPC& q) { dma16(q.m0w + (T % 6) * 0x4000u + 1024u * Q, q.vwsrc, q.srdW, q.sofw + T * 64u * 8192u + 8192u * 8u * (Q >> 1) + 128u * (Q & 1)); }
template <int T, int J> DI void qp_x(QPC& q) {
  constexpr int a = 128 + 32 * (T & 3) + 4 * J;
  asm volatile("buffer_load_dwordx4 a[%c3:%c4], %0, %1, %2 offen offset:%c5" :: "v"(q.vx), "s"(q.srdX), "s"((unsigned)((J >> 2) * 40 * 1024 + T * 4096)), "i"(a), "i"(a + 3), "i"((J & 3) * 1024) : "memory", ACLOB_HI);
}
template <int T> DI void qp_wt(QPC& q) { qp_w<T, 0>(q); qp_w<T, 1>(q); qp_w<T, 2>(q); qp_w<T, 3>(q); }
template <int T> DI void qp_xt(QPC& q) { qp_x<T, 0>(q); qp_x<T, 1>(q); qp_x<T, 2>(q); qp_x<T, 3>(q); qp_x<T, 4>(q); qp_x<T, 5>(q); qp_x<T, 6>(q); qp_x<T, 7>(q); }
#define QP_L(P) "+v"(q.wl[P][0][0]), "+v"(q.wl[P][0][1]), "+v"(q.wl[P][0][2]), "+v"(q.wl[P][0][3]), "+v"(q.wl[P][1][0]), "+v"(q.wl[P][1][1]), "+v"(q.wl[P][1][2]), "+v"(q.wl[P][1][3]), \
                "+v"(q.wl[P][2][0]), "+v"(q.wl[P][2][1]), "+v"(q.wl[P][2][2]), "+v"(q.wl[P][2][3]), "+v"(q.wl[P][3][0]), "+v"(q.wl[P][3][1]), "+v"(q.wl[P][3][2]), "+v"(q.wl[P][3][3])
#define QP_U(P) "+v"(q.wu[P][0][0]), "+v"(q.wu[P][0][1]), "+v"(q.wu[P][0][2]), "+v"(q.wu[P][0][3]), "+v"(q.wu[P][1][0]), "+v"(q.wu[P][1][1]), "+v"(q.wu[P][1][2]), "+v"(q.wu[P][1][3]), \
                "+v"(q.wu[P][2][0]), "+v"(q.wu[P][2][1]), "+v"(q.wu[P][2][2]), "+v"(q.wu[P][2][3]), "+v"(q.wu[P][3][0]), "+v"(q.wu[P][3][1]), "+v"(q.wu[P][3][2]), "+v"(q.wu[P][3][3])
template <int T, int M> DI void qp_gap(QPC& q, W4C& c, unsigned vrd) {
  constexpr int ks = M >> 3, db = (M >> 1) & 3, qb = M & 1, ao = AO_(db, qb), ax = 128 + 32 * (T & 3) + 16 * qb + 4 * ks, PAR = T & 1;
  const bf16x8 wf = __builtin_shufflevector(q.wl[PAR][db][ks], q.wu[PAR][db][ks], 0, 1, 2, 3, 4, 5, 6, 7);
  if constexpr (T == 0 && ks == 0) asm volatile("s_nop 1\n\tv_mfma_f32_32x32x16_bf16 a[%c1:%c2], %0, a[%c3:%c4], 0" :: "v"(wf), "i"(ao), "i"(ao + 15), "i"(ax), "i"(ax + 3) : ACLOB);
  else                           asm volatile("v_mfma_f32_32x32x16_bf16 a[%c1:%c2], %0, a[%c3:%c4], a[%c1:%c2]" :: "v"(wf), "i"(ao), "i"(ao + 15), "i"(ax), "i"(ax + 3) : ACLOB);
  if constexpr (T < 9 && M < 16) { qp_tr<PAR ^ 1, 2 * M>(q, vrd); qp_tr<PAR ^ 1, 2 * M + 1>(q, vrd); }
  if constexpr (T <= 3 && M >= 16 && M < 20) qp_w<T + 6, M - 16>(q);
  if constexpr (T >= 1 && T <= 6 && M >= 20 && M < 28) qp_x<T + 3, M - 20>(q);
  if constexpr (T == 4 && M >= 28) dma16(c.m0k + 1024u * (M - 28), c.vksrc[M & 1], c.srdK, c.sofk + KRS * 4u * (M - 28));
  if constexpr (T == 5 && M >= 16 && M < 20) dma16(c.m0v + 1024u * (M - 16), c.vvsrc, c.srdV, c.sofv + KRS * 8u * ((M - 16) >> 1) + 128u * (M & 1));
  if constexpr (T == 5 && M >= 28) dma16(c.m0k + 0x4000u + 1024u * (M - 28), c.vksrc[M & 1], c.srdK, KTILEB + c.sofk + KRS * 4u * (M - 28));
  if constexpr (T == 9 && M >= 1 && M <= 16) k_read<M - 1>(c, 0u);
  GAP();
}
template <int T, int VM, int... Ms> DI void qp_step_(QPC& q, W4C& c, iseq<Ms...>) {
  const unsigned vrd = q.vtr + ((T + 1) % 6) * 0x4000u;
  if constexpr (T & 1) { asm volatile("s_waitcnt vmcnt(%c16) lgkmcnt(0)" : QP_L(1) : "i"(VM) : "memory"); asm volatile("s_barrier" : QP_U(1) :: "memory"); }
  else                 { asm volatile("s_waitcnt vmcnt(%c16) lgkmcnt(0)" : QP_L(0) : "i"(VM) : "memory"); asm volatile("s_barrier" : QP_U(0) :: "memory"); }
  GAP();
  (qp_gap<T, Ms>(q, c, vrd), ...);
}
template <int T, int VM> DI void qp_step(QPC& q, W4C& c) { qp_step_<T, VM>(q, c, make_iseq<32>{}); }
template <int... Is> DI void qp_tr0(QPC& q, iseq<Is...>) { (qp_tr<0, Is>(q, q.vtr), ...); }
template <int R> DI u32x4 acc_pack8(float f) {
  float o[8];
  asm volatile("v_accvgpr_read_b32 %0, a[%c8]\n\tv_accvgpr_read_b32 %1, a[%c9]\n\tv_accvgpr_read_b32 %2, a[%c10]\n\tv_accvgpr_read_b32 %3, a[%c11]\n\t"
               "v_accvgpr_read_b32 %4, a[%c12]\n\tv_accvgpr_read_b32 %5, a[%c13]\n\tv_accvgpr_read_b32 %6, a[%c14]\n\tv_accvgpr_read_b32 %7, a[%c15]"
               : "=v"(o[0]), "=v"(o[1]), "=v"(o[2]), "=v"(o[3]), "=v"(o[4]), "=v"(o[5]), "=v"(o[6]), "=v"(o[7])
               : "i"(R), "i"(R + 1), "i"(R + 2), "i"(R + 3), "i"(R + 4), "i"(R + 5), "i"(R + 6), "i"(R + 7));
  u32x4 w;
#pragma unroll
  for (int e = 0; e < 4; ++e) w[e] = cvtpk(o[2 * e] * f, o[2 * e + 1] * f);
  return w;
}
template <int I> DI void q_conv1(float C) {
  constexpr int qb = I >> 3, d0 = I & 7, aq = AQ_(qb, d0);
  const u32x4 w = acc_pack8<AO_(d0 >> 1, qb) + 8 * (d0 & 1)>(C);
  asm volatile("v_accvgpr_write_b32 a[%c4], %0\n\tv_accvgpr_write_b32 a[%c5], %1\n\tv_accvgpr_write_b32 a[%c6], %2\n\tv_accvgpr_write_b32 a[%c7], %3"
               :: "v"(w[0]), "v"(w[1]), "v"(w[2]), "v"(w[3]), "i"(aq), "i"(aq + 1), "i"(aq + 2), "i"(aq + 3) : ACLOB_HI);
}
template <int... Is> DI void q_conv(float C, iseq<Is...>) { (q_conv1<Is>(C), ...); }
template <int I> DI void o_store1(unsigned char* dst, float il0, float il1) {
  constexpr int qb = I >> 3, j = I & 7;
  const u32x4 w = acc_pack8<AO_(j >> 1, qb) + 8 * (j & 1)>(qb ? il1 : il0);
  *(u32x4*)(dst + (size_t)qb * 256u * 1024u + j * 1024u) = w;
}
template <int... Is> DI void o_store(unsigned char* dst, float il0, float il1, iseq<Is...>) { (o_store1<Is>(dst, il0, il1), ...); }
DI void gload16(u32x4& r, const void* p) { asm volatile("global_load_dwordx4 %0, %1, off" : "=v"(r) : "v"(p) : "memory"); }
DI void gstore16(void* p, const u32x4& r) { asm volatile("global_store_dwordx4 %0, %1, off\n\ts_nop 1" :: "v"(p), "v"(r) : "memory"); }

__global__ __launch_bounds__(256, 1)
void k_attn(const unsigned char* __restrict__ xnF, const unsigned short* __restrict__ wq, const unsigned short* __restrict__ Kc, const unsigned short* __restrict__ Vc,
            unsigned char* __restrict__ OF, const unsigned char* __restrict__ zF, unsigned char* __restrict__ z2nT,
            const unsigned short* __restrict__ w_sp, unsigned char* __restrict__ wspF) {
  extern __shared__ __attribute__((aligned(16))) char lds[];
  const unsigned tid = threadIdx.x, lane = tid & 63u, wid = rfl(tid >> 6), l32 = lane & 31u, hi = lane >> 5;
  constexpr int T = NTILE;
  const float C = 0.12751743082f;
  const unsigned xcd = blockIdx.x & 7u, idx = blockIdx.x >> 3;
  const unsigned h = 8u * (xcd >> 1) + (idx & 7u), y = 4u * (xcd & 1u) + (idx >> 3);
  const unsigned rb0 = 8u * y + 2u * wid;
  W4C c; QPC q;
  c.ldsb = (unsigned)(size_t)lds;
#pragma unroll
  for (int j = 0; j < 4; ++j) c.vkb[j] = c.ldsb + KRING + 256u * l32 + 16u * ((2u * j + hi) ^ (l32 & 7u));
  c.vvb = c.ldsb + VRING + tr_lane<false, false>(lane, 0u);
#pragma unroll
  for (int par = 0; par < 2; ++par) c.vksrc[par] = KRS * (lane >> 4) + 16u * ((lane & 15u) ^ (4u * par + (lane >> 4)));
  c.vvsrc = KRS * ((lane >> 2) & 7u) + 64u * hi + 16u * (lane & 3u);
  c.m0k = rfl(c.ldsb + KRING + 4096u * wid); c.m0v = rfl(c.ldsb + VRING + 4096u * wid);
  c.sofk = KRS * 16u * wid; c.sofv = KRS * 16u * wid;
  c.srdK = mk_srd((const char*)Kc + (size_t)h * SEQ * 256u, SEQ * 256u);
  c.srdV = mk_srd((const char*)Vc + (size_t)h * SEQ * 256u, SEQ * 256u);
  q.vtr = c.ldsb + WRING + tr_lane<true, true>(lane, 2048u);
  q.vwsrc = 8192u * ((lane >> 2) & 7u) + 64u * hi + 16u * (lane & 3u);
  q.m0w = rfl(c.ldsb + WRING + 4096u * wid); q.sofw = 8192u * 16u * wid;
  q.vx = rb0 * 40u * 1024u + lane * 16u;
  q.srdW = mk_srd((const char*)wq + h * 256u, DM * 8192u - h * 256u);
  q.srdX = mk_srd(xnF, SEQ * DM * 2u);
  const unsigned srow = blockIdx.x * 8u + 2u * wid + hi;
  u32x4 zv[4], wv[8];
#pragma unroll
  for (int i = 0; i < 4; ++i) { const unsigned pc = l32 + 32u * i, p = pc < 108u ? pc : 107u;
    gload16(zv[i], zF + ((size_t)((srow >> 5) * 108u + 54u + (p >> 1)) * 64u + (p & 1u) * 32u + (srow & 31u)) * 16u); }
#pragma unroll
  for (int i = 0; i < 8; ++i) gload16(wv[i], (const char*)w_sp + (size_t)srow * 4096u + (l32 + 32u * i) * 16u);
  asm volatile("s_nop 4" ::: "memory");
  qp_wt<0>(q); qp_xt<0>(q); qp_wt<1>(q); qp_xt<1>(q); qp_wt<2>(q); qp_xt<2>(q); qp_wt<3>(q); qp_xt<3>(q);
  asm volatile("s_waitcnt vmcnt(48)" : "+v"(zv[0]), "+v"(zv[1]), "+v"(zv[2]), "+v"(zv[3]), "+v"(wv[0]), "+v"(wv[1]), "+v"(wv[2]), "+v"(wv[3]), "+v"(wv[4]), "+v"(wv[5]), "+v"(wv[6]), "+v"(wv[7]) :: "memory");
#pragma unroll
  for (int i = 0; i < 8; ++i) { const unsigned ch = l32 + 32u * i;
    gstore16(wspF + ((size_t)((srow >> 5) * 128u + (ch >> 1)) * 64u + (ch & 1u) * 32u + (srow & 31u)) * 16u, wv[i]); }
  {
    const bool v3 = l32 < 12u;
    float f[32], s = 0.f;
#pragma unroll
    for (int i = 0; i < 4; ++i)
#pragma unroll
      for (int e = 0; e < 4; ++e) { f[8 * i + 2 * e] = bflo(zv[i][e]); f[8 * i + 2 * e + 1] = bfhi(zv[i][e]); const float t2 = f[8 * i + 2 * e] + f[8 * i + 2 * e + 1]; s += (i < 3 || v3) ? t2 : 0.f; }
#pragma unroll
    for (int o = 16; o; o >>= 1) s += __shfl_xor(s, o);
    const float mean = s * (1.f / FH); float qq = 0.f;
#pragma unroll
    for (int i = 0; i < 32; ++i) { f[i] -= mean; qq += (i < 24 || v3) ? f[i] * f[i] : 0.f; }
#pragma unroll
    for (int o = 16; o; o >>= 1) qq += __shfl_xor(qq, o);
    const float rstd = 1.f / sqrtf(qq * (1.f / FH) + 1e-5f);
#pragma unroll
    for (int i = 0; i < 4; ++i) { const unsigned pc = l32 + 32u * i, p = pc < 108u ? pc : 107u;
      u32x4 w;
#pragma unroll
      for (int e = 0; e < 4; ++e) w[e] = cvtpk(f[8 * i + 2 * e] * rstd, f[8 * i + 2 * e + 1] * rstd);
      gstore16(z2nT + ((size_t)(blockIdx.x * 27u + (p >> 2)) * 8u + (srow & 7u)) * 64u + (p & 3u) * 16u, w); }
  }
  qp_wt<4>(q); qp_wt<5>(q);
  wait_vm_bar<63>();
  qp_tr0(q, make_iseq<32>{});
  qp_step<0, 52>(q, c); qp_step<1, 44>(q, c); qp_step<2, 44>(q, c); qp_step<3, 32>(q, c); qp_step<4, 24>(q, c);
  qp_step<5, 24>(q, c); qp_step<6, 24>(q, c); qp_step<7, 24>(q, c); qp_step<8, 12>(q, c); qp_step<9, 0>(q, c);
  asm volatile("s_nop 7\n\ts_nop 7" ::: "memory");
  q_conv(C, make_iseq<16>{});
  asm volatile("s_waitcnt lgkmcnt(0)\n\ts_nop 1" ::: "memory");
  GAP();
  c.mhat[0] = c.mhat[1] = 0.f; c.lsum[0] = c.lsum[1] = 0.f; c.fct[0] = c.fct[1] = 1.f; c.pend = false;
  phaseA<0, true>(c, 0);  phaseB<0, true, false>(c, 0);
  phaseA<1, false>(c, 1); phaseB<1, false, true>(c, 1);
  for (int t = 2; t < T - 2; t += 2) {
    phaseA<0, false>(c, t);     phaseB<0, false, false>(c, t);
    phaseA<1, false>(c, t + 1); phaseB<1, false, false>(c, t + 1);
  }
  phaseA<0, false>(c, T - 2); phaseB<0, false, false, false, true, true>(c, T - 2);
  phaseA<1, false>(c, T - 1); phaseB<1, false, false, false, false, false>(c, T - 1);
  {
    const unsigned vslot = (unsigned)((T - 1) & 1) * 0x4000u;
    tr_reads(c, vslot, make_iseq<32>{});
#pragma unroll
    for (int e = 0; e < 32; ++e) c.S[1][1][e >> 4][e & 15] = __builtin_amdgcn_exp2f(c.S[1][1][e >> 4][e & 15]);
    packs<1>(c, make_iseq<32>{});
    rowsums<1>(c, iseq<8, 9, 10, 11, 12, 13, 14, 15, 24, 25, 26, 27, 28, 29, 30, 31>{});
    lsum_merge(c);
    asm volatile("s_waitcnt lgkmcnt(0)" : W4_VLO :: "memory"); asm volatile("" : W4_VUP :: "memory");
    GAP();
    v_concat(c);
    asm volatile("s_nop 1" ::: "memory");
    pv_all(c, make_iseq<32>{});
    asm volatile("s_nop 7\n\ts_nop 7" ::: "memory");
  }
  {
    unsigned lb; asm volatile("v_mbcnt_lo_u32_b32 %0, -1, 0\n\tv_mbcnt_hi_u32_b32 %0, -1, %0" : "=v"(lb));
    const float il0 = __builtin_amdgcn_rcpf(c.lsum[0]), il1 = __builtin_amdgcn_rcpf(c.lsum[1]);
    o_store(OF + ((size_t)(rb0 * 256u + 8u * h) * 64u + lb) * 16u, il0, il1, make_iseq<16>{});
  }
}

extern "C" void kernel_launch(void* const* d_in, const int* in_sizes, int n_in, void* d_out, int out_size, void* d_ws, size_t ws_size, hipStream_t stream) {
  const unsigned short* x = (const unsigned short*)d_in[0];
  const unsigned short* Kc = (const unsigned short*)d_in[1];
  const unsigned short* Vc = (const unsigned short*)d_in[2];
  const unsigned short* wq = (const unsigned short*)d_in[3];
  const unsigned short* wo = (const unsigned short*)d_in[4];
  const unsigned short* w_in = (const unsigned short*)d_in[6];
  const unsigned short* w_sp = (const unsigned short*)d_in[7];
  const unsigned short* b_sp = (const unsigned short*)d_in[8];
  const unsigned short* w_out = (const unsigned short*)d_in[9];
  unsigned char* ws = (unsigned char*)d_ws;
  (void)hipFuncSetAttribute((const void*)k_attn, hipFuncAttributeMaxDynamicSharedMemorySize, 163840);
  (void)hipFuncSetAttribute((const void*)k_zgemm, hipFuncAttributeMaxDynamicSharedMemorySize, 81920);
  (void)hipFuncSetAttribute((const void*)k_mid, hipFuncAttributeMaxDynamicSharedMemorySize, 81920);
  hipLaunchKernelGGL(k_ln, dim3(256), dim3(256), 0, stream, x, ws + WS_XN);
  hipLaunchKernelGGL(k_zgemm, dim3(224), dim3(256), 81920, stream, ws + WS_XN, w_in, ws + WS_Z);
  hipLaunchKernelGGL(k_attn, dim3(256), dim3(256), 163840, stream, ws + WS_XN, wq, Kc, Vc, ws + WS_O, ws + WS_Z, ws + WS_Z2N, w_sp, ws + WS_WSP);
  hipLaunchKernelGGL(k_mid, dim3(256), dim3(256), 81920, stream, ws + WS_O, wo, ws + WS_SP, ws + WS_WSP, ws + WS_Z2N, ws + WS_Z, b_sp, ws + WS_U);
  hipLaunchKernelGGL(k_out, dim3(160), dim3(256), 49152, stream, ws + WS_U, w_out, ws + WS_SP, x, (unsigned short*)d_out);
}
```

```cpp
#include <hip/hip_runtime.h>
#include <hip/hip_bf16.h>
#include <utility>

typedef float    f32x16 __attribute__((ext_vector_type(16)));
typedef float    f32x2  __attribute__((ext_vector_type(2)));
typedef float    f32x4  __attribute__((ext_vector_type(4)));
typedef __bf16   bf16x8 __attribute__((ext_vector_type(8)));
typedef __bf16   bf16x4 __attribute__((ext_vector_type(4)));
typedef __bf16   bf16x2 __attribute__((ext_vector_type(2)));
typedef unsigned u32x4  __attribute__((ext_vector_type(4)));
typedef unsigned u32x2  __attribute__((ext_vector_type(2)));
template <int... Is> using iseq = std::integer_sequence<int, Is...>;
template <int N> using make_iseq = std::make_integer_sequence<int, N>;
typedef __amdgpu_buffer_rsrc_t rsrc_t;

#define DI __device__ __forceinline__
#define GAP() __builtin_amdgcn_sched_barrier(0)
#define SRD_WORD3 0x00020000
#define A10(x) "a" #x "0", "a" #x "1", "a" #x "2", "a" #x "3", "a" #x "4", "a" #x "5", "a" #x "6", "a" #x "7", "a" #x "8", "a" #x "9"
#define ACLOB_LO "a0", "a1", "a2", "a3", "a4", "a5", "a6", "a7", "a8", "a9", A10(1), A10(2), A10(3), A10(4), A10(5), A10(6), A10(7), A10(8), A10(9), A10(10), A10(11), \
                 "a120", "a121", "a122", "a123", "a124", "a125", "a126", "a127"
#define ACLOB_HI "a128", "a129", A10(13), A10(14), A10(15), A10(16), A10(17), A10(18), A10(19), A10(20), A10(21), A10(22), A10(23), A10(24), \
                 "a250", "a251", "a252", "a253", "a254", "a255"
#define ACLOB ACLOB_LO, ACLOB_HI

constexpr int SEQ = 2048, DM = 640, NH = 32, DH = 128, FF = 1728, FH = 864;
constexpr size_t WS_XN  = 0;
constexpr size_t WS_Z   = WS_XN  + (size_t)SEQ * DM * 2;
constexpr size_t WS_O   = WS_Z   + (size_t)SEQ * FF * 2;
constexpr size_t WS_Z2N = WS_O   + (size_t)SEQ * NH * DH * 2;
constexpr size_t WS_WSP = WS_Z2N + (size_t)SEQ * FH * 2;
constexpr size_t WS_SP  = WS_WSP + (size_t)SEQ * SEQ * 2;
constexpr size_t WS_U   = WS_SP  + (size_t)2 * SEQ * DM * 4;
constexpr size_t WS_END = WS_U   + (size_t)SEQ * FH * 2;

DI unsigned rfl(unsigned x) { return __builtin_amdgcn_readfirstlane(x); }
DI unsigned cvtpk(float lo, float hi) { f32x2 v = {lo, hi}; bf16x2 b = __builtin_convertvector(v, bf16x2); return __builtin_bit_cast(unsigned, b); }
DI float bflo(unsigned w) { return __uint_as_float(w << 16); }
DI float bfhi(unsigned w) { return __uint_as_float(w & 0xffff0000u); }
DI rsrc_t mk_srd(const void* p, unsigned bytes) { return __builtin_amdgcn_make_buffer_rsrc((void*)p, (short)0, (int)bytes, SRD_WORD3); }
DI float max3f(float a, float b, float c) { float r; asm("v_max3_f32 %0, %1, %2, %3" : "=v"(r) : "v"(a), "v"(b), "v"(c)); return r; }
DI float max2f(float a, float b) { float r; asm("v_max_f32_e32 %0, %1, %2" : "=v"(r) : "v"(a), "v"(b)); return r; }
DI float fadd_s(float a, float b) { float r; asm("v_add_f32_e32 %0, %1, %2" : "=v"(r) : "v"(a), "v"(b)); return r; }
DI float fmul_s(float a, float b) { float r; asm("v_mul_f32_e32 %0, %1, %2" : "=v"(r) : "v"(a), "v"(b)); return r; }
DI float fsub_s(float a, float b) { float r; asm("v_sub_f32_e32 %0, %1, %2" : "=v"(r) : "v"(a), "v"(b)); return r; }
template <int N> DI void wait_vm_bar() { asm volatile("s_waitcnt vmcnt(%c0) lgkmcnt(0)\n\ts_barrier" :: "i"(N > 63 ? 63 : N) : "memory"); }
DI void dma16(unsigned m0v, unsigned voff, rsrc_t srd, unsigned soff) {
  asm volatile("s_mov_b32 m0, %0\n\ts_nop 0\n\tbuffer_load_dwordx4 %1, %2, %3 offen lds" :: "s"(m0v), "v"(voff), "s"(srd), "s"(soff) : "m0", "memory");
}
template <bool NATK, bool SWAPN> DI unsigned tr_lane(unsigned lane, unsigned halfstride) {
  const unsigned c = lane & 3u, cs = SWAPN ? (((c & 1u) << 1) | (c >> 1)) : c, hi = lane >> 5;
  return (cs << 3) | (((lane >> 2) & 3u) << 6) | (((lane >> 4) & 1u) << 5) | (NATK ? hi * halfstride : (hi << 8));
}

__global__ __launch_bounds__(256) void k_ln(const unsigned short* __restrict__ x, unsigned char* __restrict__ xnF) {
  const unsigned lane = threadIdx.x & 63u, wid = threadIdx.x >> 6, l = lane & 31u;
  const unsigned row = blockIdx.x * 8u + wid * 2u + (lane >> 5);
  const u32x4* src = (const u32x4*)(x + (size_t)row * DM);
  u32x4 v[3]; v[0] = src[l]; v[1] = src[l + 32]; v[2] = (l < 16u) ? src[l + 64] : (u32x4){0u, 0u, 0u, 0u};
  float f[24]; float s = 0.f;
#pragma unroll
  for (int i = 0; i < 3; ++i)
#pragma unroll
    for (int e = 0; e < 4; ++e) { f[8 * i + 2 * e] = bflo(v[i][e]); f[8 * i + 2 * e + 1] = bfhi(v[i][e]); s += f[8 * i + 2 * e] + f[8 * i + 2 * e + 1]; }
#pragma unroll
  for (int o = 16; o; o >>= 1) s += __shfl_xor(s, o);
  const float mean = s * (1.f / DM); float q = 0.f;
#pragma unroll
  for (int i = 0; i < 24; ++i) { f[i] -= mean; if (i < 16 || l < 16u) q += f[i] * f[i]; }
#pragma unroll
  for (int o = 16; o; o >>= 1) q += __shfl_xor(q, o);
  const float rstd = 1.f / sqrtf(q * (1.f / DM) + 1e-5f);
#pragma unroll
  for (int i = 0; i < 3; ++i) {
    const unsigned c = l + 32u * i;
    if (i < 2 || l < 16u) {
      u32x4 w;
#pragma unroll
      for (int e = 0; e < 4; ++e) w[e] = cvtpk(f[8 * i + 2 * e] * rstd, f[8 * i + 2 * e + 1] * rstd);
      *(u32x4*)(xnF + ((size_t)((row >> 5) * 40u + (c >> 1)) * 64u + (c & 1u) * 32u + (row & 31u)) * 16u) = w;
    }
  }
}

constexpr int LA = 6;
template <int NT, int KS> struct GemmC {
  static constexpr int P = NT * KS / 4, TILEB = NT * KS * 1024, G = P + KS, NM = NT * KS;
  unsigned vw[P];
  unsigned vx[2];
  unsigned vtr;
  unsigned m0w;
  unsigned wstride;
  rsrc_t srdW, srdX;
  f32x16 acc[NT];
};
struct NoEx { static constexpr int N = 0; template <int J> DI void issue() {} };
template <int KS> constexpr int GX(int u, int ks) { return 32 + (u * KS + ks) * 4; }
template <int NT, int KS, bool NATK, int AS, int F> DI void g_tr(unsigned vaddr) {
  constexpr int ks = F / NT, nt = F % NT, a = 4 * AS;
  constexpr int o0 = NATK ? ((2 * ks) * NT + nt) * 512 : ((2 * ks) * NT + nt) * 512, o1 = NATK ? o0 + 256 : o0 + NT * 512;
  asm volatile("ds_read_b64_tr_b16 a[%c1:%c2], %0 offset:%c5\n\tds_read_b64_tr_b16 a[%c3:%c4], %0 offset:%c6" :: "v"(vaddr), "i"(a), "i"(a + 1), "i"(a + 2), "i"(a + 3), "i"(o0), "i"(o1) : "memory", ACLOB);
}
template <int NT, int KS, int XI, int KSI> DI void g_xload(GemmC<NT, KS>& c, unsigned soff) {
  constexpr int a = GX<KS>(XI, KSI);
  asm volatile("buffer_load_dwordx4 a[%c3:%c4], %0, %1, %2 offen offset:%c5" :: "v"(c.vx[KSI >> 2]), "s"(c.srdX), "s"(soff), "i"(a), "i"(a + 3), "i"((KSI & 3) * 1024) : "memory", ACLOB);
}
template <int NT, int KS, int AS, int XI, bool ZERO, int LG, int M> DI void g_mfma(GemmC<NT, KS>& c) {
  constexpr int ks = M / NT, nt = M % NT, a = 4 * AS, b = GX<KS>(XI, ks);
  if constexpr (ZERO && ks == 0) asm volatile("s_waitcnt lgkmcnt(%c5)\n\tv_mfma_f32_32x32x16_bf16 %0, a[%c1:%c2], a[%c3:%c4], 0" : "=v"(c.acc[nt]) : "i"(a), "i"(a + 3), "i"(b), "i"(b + 3), "i"(LG) : "memory", ACLOB);
  else asm volatile("s_waitcnt lgkmcnt(%c5)\n\tv_mfma_f32_32x32x16_bf16 %0, a[%c1:%c2], a[%c3:%c4], %0" : "+v"(c.acc[nt]) : "i"(a), "i"(a + 3), "i"(b), "i"(b + 3), "i"(LG) : "memory", ACLOB);
}
template <int NT, int KS, int SLOT, int Q> DI void g_vmem(GemmC<NT, KS>& c, unsigned sw, unsigned sx) {
  constexpr int P = GemmC<NT, KS>::P;
  if constexpr (Q < P) dma16(c.m0w + SLOT * GemmC<NT, KS>::TILEB + 1024u * Q, c.vw[Q], c.srdW, sw);
  else g_xload<NT, KS, SLOT, Q - P>(c, sx);
}
template <int NT, int KS, int SLOT, int... Q> DI void g_group(GemmC<NT, KS>& c, unsigned g, iseq<Q...>) { (g_vmem<NT, KS, SLOT, Q>(c, g * c.wstride, g * (KS * 1024u)), ...); }
template <int NT, int KS, int... Gs> DI void g_pre(GemmC<NT, KS>& c, iseq<Gs...>) { (g_group<NT, KS, Gs>(c, Gs, make_iseq<GemmC<NT, KS>::G>{}), ...); }
template <int NT, int KS, bool NATK, int D, int PRE, int TM, bool FIRST, bool LAST, bool LD, bool EXS, class EX, int J>
DI void g_gap(GemmC<NT, KS>& c, EX& ex, unsigned vrd, unsigned sw, unsigned sx) {
  constexpr int NM = NT * KS, G = GemmC<NT, KS>::G, T1 = (TM + 1) % D;
  if constexpr (J < LA) { if constexpr (!FIRST) g_mfma<NT, KS, (TM * NM + NM - LA + J) & 7, TM, false, (LAST ? 2 * (LA - 1 - J) : 2 * (LA - 1)), NM - LA + J>(c); }
  else if constexpr (!LAST) g_mfma<NT, KS, (T1 * NM + J - LA) & 7, T1, FIRST, 2 * (LA - 1), J - LA>(c);
  if constexpr (!LAST) g_tr<NT, KS, NATK, (T1 * NM + J) & 7, J>(vrd);
  if constexpr (LD && J < G) g_vmem<NT, KS, (TM + 1 + PRE) % D, J>(c, sw, sx);
  if constexpr (EXS) { if constexpr (2 * J < EX::N) ex.template issue<2 * J>(); if constexpr (2 * J + 1 < EX::N) ex.template issue<2 * J + 1>(); }
  GAP();
}
template <int NT, int KS, bool NATK, int D, int PRE, int TM, bool FIRST, bool LAST, bool LD, bool EXS, int VM, class EX, int... Js>
DI void g_step_(GemmC<NT, KS>& c, EX& ex, int t, iseq<Js...>) {
  const unsigned vrd = c.vtr + ((TM + 1) % D) * GemmC<NT, KS>::TILEB;
  const unsigned sw = (unsigned)(t + 1 + PRE) * c.wstride, sx = (unsigned)(t + 1 + PRE) * (KS * 1024u);
  if constexpr (!LAST) asm volatile("s_waitcnt vmcnt(%c0)\n\ts_barrier" :: "i"(VM > 63 ? 63 : VM) : "memory");
  GAP();
  (g_gap<NT, KS, NATK, D, PRE, TM, FIRST, LAST, LD, EXS, EX, Js>(c, ex, vrd, sw, sx), ...);
}
template <int NT, int KS, bool NATK, int NTILES, int D, int PRE, class EX, int T> DI void g_step_static(GemmC<NT, KS>& c, EX& ex) {
  constexpr int G = GemmC<NT, KS>::G, TEX = NTILES - 1 - PRE;
  constexpr int last = (T + PRE < NTILES - 1) ? T + PRE : NTILES - 1;
  constexpr int VM = (last - (T + 1)) * G + (T > TEX ? EX::N : 0);
  g_step_<NT, KS, NATK, D, PRE, (T + D) % D, (T == -1), (T == NTILES - 1), (T + 1 + PRE < NTILES), (T == TEX), VM, EX>(c, ex, T, make_iseq<(T == NTILES - 1) ? LA : NT * KS>{});
}
template <int NT, int KS, bool NATK, int NTILES, int D, int PRE, class EX, int T0, int... Ts> DI void g_tail(GemmC<NT, KS>& c, EX& ex, iseq<Ts...>) {
  (g_step_static<NT, KS, NATK, NTILES, D, PRE, EX, T0 + Ts>(c, ex), ...);
}
template <int NT, int KS, bool NATK, int D, int PRE, class EX, int... Ts> DI void g_body(GemmC<NT, KS>& c, EX& ex, int t, iseq<Ts...>) {
  (g_step_<NT, KS, NATK, D, PRE, Ts, false, false, true, false, (PRE - 1) * GemmC<NT, KS>::G, EX>(c, ex, t + Ts, make_iseq<NT * KS>{}), ...);
}
template <int NT, int KS, bool NATK, int NTILES, int D, int PRE, class EX> DI void gemm_run(GemmC<NT, KS>& c, EX& ex) {
  static_assert(NTILES <= D || (PRE <= D - 2 && (D * NT * KS) % 8 == 0), "ring");
  static_assert(32 + D * KS * 4 <= 256 && 2 * NT * KS >= EX::N && NTILES - 1 - PRE >= -1, "regs");
  asm volatile("s_nop 4" ::: "memory");
  g_pre<NT, KS>(c, make_iseq<PRE>{});
  if constexpr (NTILES <= D) g_tail<NT, KS, NATK, NTILES, D, PRE, EX, -1>(c, ex, make_iseq<NTILES + 1>{});
  else {
    constexpr int T0 = D * ((NTILES - 1 - PRE) / D);
    g_step_static<NT, KS, NATK, NTILES, D, PRE, EX, -1>(c, ex);
    for (int t = 0; t < T0; t += D) g_body<NT, KS, NATK, D, PRE, EX>(c, ex, t, make_iseq<D>{});
    g_tail<NT, KS, NATK, NTILES, D, PRE, EX, T0>(c, ex, make_iseq<NTILES - T0>{});
  }
  asm volatile("s_waitcnt vmcnt(0)\n\ts_nop 7\n\ts_nop 7" ::: "memory");
#pragma unroll
  for (int nt = 0; nt < NT; ++nt) asm volatile("" : "+v"(c.acc[nt]));
  GAP();
}
DI void bload16(u32x4& r, unsigned voff, rsrc_t srd, unsigned soff) { asm volatile("buffer_load_dwordx4 %0, %1, %2, %3 offen" : "=v"(r) : "v"(voff), "s"(srd), "s"(soff) : "memory"); }
template <int NT, int KS, bool NATK, bool SWAPN> DI void gemm_setup_rm(GemmC<NT, KS>& c, unsigned ldsb, unsigned lane, unsigned wid, unsigned ldB) {
  constexpr int P = GemmC<NT, KS>::P;
#pragma unroll
  for (int q = 0; q < P; ++q) { const unsigned p = wid * P + q, kg = p / (NT / 2), cp = p % (NT / 2);
    c.vw[q] = ldB * (8u * kg + ((lane >> 2) & 7u)) + 128u * cp + 64u * (lane >> 5) + 16u * (lane & 3u); }
  c.wstride = ldB * 16u * KS;
  c.m0w = rfl(ldsb + 1024u * P * wid);
  c.vtr = ldsb + tr_lane<NATK, SWAPN>(lane, NT * 512u);
}

DI float gelu_tanh(float v) {
  const float y2 = v * (-2.3022081985f - 0.1029432397f * v * v);
  return v * __builtin_amdgcn_rcpf(1.f + __builtin_amdgcn_exp2f(y2));
}
__global__ __launch_bounds__(256, 1) void k_zgemm(const unsigned char* __restrict__ xnF, const unsigned short* __restrict__ w_in, unsigned char* __restrict__ zF) {
  extern __shared__ __attribute__((aligned(16))) char lds[];
  const unsigned lane = threadIdx.x & 63u, wid = rfl(threadIdx.x >> 6);
  const unsigned xcd = blockIdx.x & 7u, idx = blockIdx.x >> 3;
  const unsigned rb128 = 4u * (xcd >> 1) + idx / 7u, cb = 7u * (xcd & 1u) + idx % 7u, rb = 4u * rb128 + wid;
  GemmC<4, 5> c;
  gemm_setup_rm<4, 5, true, false>(c, (unsigned)(size_t)lds, lane, wid, FF * 2u);
  c.srdW = mk_srd((const char*)w_in + cb * 256u, DM * FF * 2u - cb * 256u);
  c.srdX = mk_srd(xnF, SEQ * DM * 2u);
  c.vx[0] = rb * 40u * 1024u + lane * 16u; c.vx[1] = c.vx[0] + 4096u;
  NoEx ex; gemm_run<4, 5, true, 8, 8, 6>(c, ex);
#pragma unroll
  for (int nt = 0; nt < 4; ++nt) {
    const unsigned ct = 4u * cb + nt;
    if (ct < 54u) {
#pragma unroll
      for (int a = 0; a < 2; ++a) {
        u32x4 w;
#pragma unroll
        for (int e = 0; e < 4; ++e) w[e] = cvtpk(gelu_tanh(c.acc[nt][8 * a + 2 * e]), gelu_tanh(c.acc[nt][8 * a + 2 * e + 1]));
        *(u32x4*)(zF + ((size_t)(rb * 108u + 2u * ct + a) * 64u + lane) * 16u) = w;
      }
    }
  }
}

template <int NT> struct ExZ { static constexpr int N = 2 * NT; u32x4 r[N]; unsigned voff; rsrc_t srd; template <int J> DI void issue() { bload16(r[J], voff, srd, J * 1024u); } };
template <int NT> DI void spatial_unit(char* lds, unsigned lane, unsigned wid, unsigned rb, unsigned ct0, const unsigned char* wspF, const unsigned char* z2nT,
                                       const unsigned char* zF, const unsigned short* b_sp, unsigned char* uF) {
  GemmC<NT, 4> c;
  const unsigned ldsb = (unsigned)(size_t)lds;
#pragma unroll
  for (int q = 0; q < NT; ++q) { const unsigned sg = 2u * (wid * NT + q) + (lane >> 5), kg = sg / NT, nt = sg % NT;
    c.vw[q] = (kg * 27u + ct0 + nt) * 512u + (lane & 31u) * 16u; }
  c.wstride = 8u * 27u * 512u;
  c.m0w = rfl(ldsb + 1024u * NT * wid);
  c.vtr = ldsb + tr_lane<true, true>(lane, NT * 512u);
  c.srdW = mk_srd(z2nT, SEQ * FH * 2u);
  c.srdX = mk_srd(wspF, SEQ * SEQ * 2u);
  c.vx[0] = rb * 128u * 1024u + lane * 16u; c.vx[1] = c.vx[0];
  const float bias = bflo((unsigned)b_sp[rb * 32u + (lane & 31u)]);
  ExZ<NT> ex; ex.voff = (rb * 108u + 2u * ct0) * 1024u + lane * 16u; ex.srd = mk_srd(zF, SEQ * FF * 2u);
  asm volatile("s_waitcnt vmcnt(0)" :: "v"(bias) : "memory");
  gemm_run<NT, 4, true, 32, 8, 6>(c, ex);
#pragma unroll
  for (int j = 0; j < 2 * NT; ++j) asm volatile("" : "+v"(ex.r[j]));
#pragma unroll
  for (int nt = 0; nt < NT; ++nt)
#pragma unroll
    for (int a = 0; a < 2; ++a) {
      const unsigned ks = 2u * (ct0 + nt) + a;
      const u32x4 z1 = ex.r[2 * nt + a];
      u32x4 w;
#pragma unroll
      for (int e = 0; e < 4; ++e) w[e] = cvtpk(bflo(z1[e]) * (c.acc[nt][8 * a + 2 * e] + bias), bfhi(z1[e]) * (c.acc[nt][8 * a + 2 * e + 1] + bias));
      *(u32x4*)(uF + ((size_t)(rb * 54u + ks) * 64u + lane) * 16u) = w;
    }
}
__global__ __launch_bounds__(256, 1) void k_mid(const unsigned char* __restrict__ OF, const unsigned short* __restrict__ wo, unsigned char* __restrict__ sP,
                                                const unsigned char* __restrict__ wspF, const unsigned char* __restrict__ z2nT, const unsigned char* __restrict__ zF,
                                                const unsigned short* __restrict__ b_sp, unsigned char* __restrict__ uF) {
  extern __shared__ __attribute__((aligned(16))) char lds[];
  const unsigned lane = threadIdx.x & 63u, wid = rfl(threadIdx.x >> 6);
  const unsigned xcd = blockIdx.x & 7u, idx = blockIdx.x >> 3;
  if (idx >= 12u) {
    const unsigned i = idx - 12u, kh = xcd & 1u, rb128 = 4u * (xcd >> 1) + i / 5u, cb = i % 5u, rb = 4u * rb128 + wid;
    GemmC<4, 4> c;
    gemm_setup_rm<4, 4, false, true>(c, (unsigned)(size_t)lds, lane, wid, DM * 2u);
    const unsigned wofs = kh * 2048u * DM * 2u + cb * 256u;
    c.srdW = mk_srd((const char*)wo + wofs, NH * DH * DM * 2u - wofs);
    c.srdX = mk_srd(OF, SEQ * NH * DH * 2u);
    c.vx[0] = (rb * 256u + kh * 128u) * 1024u + lane * 16u; c.vx[1] = c.vx[0];
    NoEx ex; gemm_run<4, 4, false, 32, 8, 6>(c, ex);
#pragma unroll
    for (int nt = 0; nt < 4; ++nt)
#pragma unroll
      for (int g = 0; g < 4; ++g) {
        f32x4 w = {c.acc[nt][4 * g], c.acc[nt][4 * g + 1], c.acc[nt][4 * g + 2], c.acc[nt][4 * g + 3]};
        *(f32x4*)(sP + ((size_t)(((kh * 64u + rb) * 20u + 4u * cb + nt) * 4u + g) * 64u + lane) * 16u) = w;
      }
  } else {
    const unsigned rb128 = 4u * (xcd >> 1) + idx / 3u, j = 3u * (xcd & 1u) + idx % 3u, rb = 4u * rb128 + wid;
    if (j & 1u) spatial_unit<4>(lds, lane, wid, rb, 15u + 4u * (j >> 1), wspF, z2nT, zF, b_sp, uF);
    else        spatial_unit<5>(lds, lane, wid, rb, 5u * (j >> 1), wspF, z2nT, zF, b_sp, uF);
  }
}

struct ExO { static constexpr int N = 20; u32x4 r[N]; unsigned vs, vxx; rsrc_t srdS, srdXx;
  template <int J> DI void issue() { if constexpr (J < 16) bload16(r[J], vs, srdS, (J >> 3) * (64u * 20u * 4096u) + (J & 7) * 1024u); else bload16(r[J], vxx, srdXx, (J - 16) * 32u); } };
__global__ __launch_bounds__(256, 1) void k_out(const unsigned char* __restrict__ uF, const unsigned short* __restrict__ w_out, const unsigned char* __restrict__ sP,
                                                const unsigned short* __restrict__ x, unsigned short* __restrict__ out) {
  extern __shared__ __attribute__((aligned(16))) char lds[];
  const unsigned lane = threadIdx.x & 63u, wid = rfl(threadIdx.x >> 6);
  const unsigned xcd = blockIdx.x & 7u, idx = blockIdx.x >> 3;
  const unsigned rb128 = 4u * (xcd >> 1) + idx / 5u, cb = 5u * (xcd & 1u) + idx % 5u, rb = 4u * rb128 + wid;
  GemmC<2, 6> c;
  gemm_setup_rm<2, 6, false, true>(c, (unsigned)(size_t)lds, lane, wid, DM * 2u);
  c.srdW = mk_srd((const char*)w_out + cb * 128u, FH * DM * 2u - cb * 128u);
  c.srdX = mk_srd(uF, SEQ * FH * 2u);
  c.vx[0] = rb * 54u * 1024u + lane * 16u; c.vx[1] = c.vx[0] + 4096u;
  const unsigned row = rb * 32u + (lane & 31u), hi = lane >> 5;
  ExO ex; ex.vs = (rb * 20u + 2u * cb) * 4096u + lane * 16u; ex.vxx = row * 1280u + cb * 128u + hi * 16u;
  ex.srdS = mk_srd(sP, 2u * SEQ * DM * 4u); ex.srdXx = mk_srd(x, SEQ * DM * 2u);
  gemm_run<2, 6, false, 9, 9, 7>(c, ex);
#pragma unroll
  for (int j = 0; j < 20; ++j) asm volatile("" : "+v"(ex.r[j]));
#pragma unroll
  for (int nt = 0; nt < 2; ++nt) {
    const unsigned ct = 2u * cb + nt;
#pragma unroll
    for (int a = 0; a < 2; ++a) {
      const size_t eo = (size_t)row * DM + 32u * ct + 16u * a + 8u * hi;
      const u32x4 xv = ex.r[16 + 2 * nt + a];
      float sv[8];
#pragma unroll
      for (int g = 0; g < 2; ++g) {
        const f32x4 p0 = __builtin_bit_cast(f32x4, ex.r[4 * nt + 2 * a + g]), p1 = __builtin_bit_cast(f32x4, ex.r[8 + 4 * nt + 2 * a + g]);
#pragma unroll
        for (int e = 0; e < 4; ++e) sv[4 * g + e] = p0[e] + p1[e];
      }
      u32x4 w;
#pragma unroll
      for (int e = 0; e < 4; ++e) {
        const float b0 = c.acc[nt][8 * a + 2 * e], b1 = c.acc[nt][8 * a + 2 * e + 1];
        const float g0 = b0 * __builtin_amdgcn_rcpf(1.f + __builtin_amdgcn_exp2f(-1.4426950408889634f * b0));
        const float g1 = b1 * __builtin_amdgcn_rcpf(1.f + __builtin_amdgcn_exp2f(-1.4426950408889634f * b1));
        w[e] = cvtpk(bflo(xv[e]) + sv[2 * e] * g0, bfhi(xv[e]) + sv[2 * e + 1] * g1);
      }
      *(u32x4*)(out + eo) = w;
    }
  }
}

constexpr int NTILE = SEQ / 64;
constexpr unsigned KRS = 256u, KTILEB = 64u * KRS;
constexpr float THR = 8.0f;
#define KRING 0x0000u
#define VRING 0x8000u
#define WRING 0x10000u
DI float w4_aone() { unsigned l; asm volatile("v_mbcnt_lo_u32_b32 %0, -1, 0\n\tv_mbcnt_hi_u32_b32 %0, -1, %0" : "=v"(l)); return l < 32u ? 1.f : 0.f; }
DI void negm_bcast(f32x16& negm, float aone, float neg_mhat) { asm volatile("s_nop 1\n\tv_mfma_f32_32x32x2_f32 %0, %1, %2, 0" : "+v"(negm) : "v"(aone), "v"(neg_mhat)); }
struct W4C {
  unsigned vkb[4], vvb, vksrc[2], vvsrc, m0k, m0v, sofk, sofv, ldsb;
  rsrc_t srdK, srdV;
  f32x16 S[2][2][2];
  f32x16 NEGM[2];
  bf16x8 Vf[4][4];
  bf16x8 Pk[2][4];
  bf16x4 vlo[4][4], vup[4][4];
  float mhat[2], lsum[2], fct[2]; bool pend;
  float rs0[2], rs1[2], pmx0[2], pmx1[2], rmax[2], delta[2];
  unsigned pm0[8], psf[8];
};
constexpr int AO_(int db, int qb) { return 16 * (2 * db + qb); }
constexpr int AQ_(int qb, int d0) { return 128 + 32 * qb + 4 * d0; }
constexpr int AK_(int kb, int d0) { return 192 + 32 * kb + 4 * d0; }
template <int I> DI void tr_read(W4C& c, unsigned vslot) {
  constexpr int ks = I >> 3, db = (I >> 1) & 3;
  if constexpr ((I & 1) == 0) asm volatile("ds_read_b64_tr_b16 %0, %1 offset:%2" : "=v"(c.vlo[db][ks]) : "v"(c.vvb + vslot), "i"(4096 * ks + 512 * db) : "memory");
  else                        asm volatile("ds_read_b64_tr_b16 %0, %1 offset:%2" : "=v"(c.vup[db][ks]) : "v"(c.vvb + vslot), "i"(4096 * ks + 2048 + 512 * db) : "memory");
}
template <int I> DI void k_read(W4C& c, unsigned slot) {
  constexpr int kb = I >> 3, d0 = I & 7;
  asm volatile("ds_read_b128 a[%c1:%c2], %0 offset:%c3" :: "v"(c.vkb[d0 & 3] + slot), "i"(AK_(kb, d0)), "i"(AK_(kb, d0) + 3), "i"(8192 * kb + 128 * (d0 >> 2)) : "memory", ACLOB_HI);
}
template <int X, bool FIRST, int M> DI void qk_mfma(W4C& c) {
  constexpr int d0 = M >> 2, kb = (M >> 1) & 1, qb = M & 1, ak = AK_(kb, d0), aq = AQ_(qb, d0);
  if constexpr (d0 == 0) {
    if constexpr (FIRST) asm volatile("v_mfma_f32_32x32x16_bf16 %0, a[%c1:%c2], a[%c3:%c4], 0" : "=&v"(c.S[X][kb][qb]) : "i"(ak), "i"(ak + 3), "i"(aq), "i"(aq + 3) : ACLOB);
    else                 asm volatile("v_mfma_f32_32x32x16_bf16 %0, a[%c2:%c3], a[%c4:%c5], %1" : "=&v"(c.S[X][kb][qb]) : "v"(c.NEGM[qb]), "i"(ak), "i"(ak + 3), "i"(aq), "i"(aq + 3) : ACLOB);
  } else asm volatile("v_mfma_f32_32x32x16_bf16 %0, a[%c1:%c2], a[%c3:%c4], %0" : "+v"(c.S[X][kb][qb]) : "i"(ak), "i"(ak + 3), "i"(aq), "i"(aq + 3) : ACLOB);
}
template <int M, bool ZERO> DI void pv_mfma(W4C& c) {
  constexpr int ks = M >> 3, db = (M >> 1) & 3, qb = M & 1, ao = AO_(db, qb);
  if constexpr (ZERO && ks == 0) asm volatile("v_mfma_f32_32x32x16_bf16 a[%c2:%c3], %0, %1, 0" :: "v"(c.Vf[db][ks]), "v"(c.Pk[qb][ks]), "i"(ao), "i"(ao + 15) : ACLOB_LO);
  else                         asm volatile("v_mfma_f32_32x32x16_bf16 a[%c2:%c3], %0, %1, a[%c2:%c3]" :: "v"(c.Vf[db][ks]), "v"(c.Pk[qb][ks]), "i"(ao), "i"(ao + 15) : ACLOB_LO);
}
template <int X, int QB, int KS, int P> DI void pack1(W4C& c) {
  u32x4 w = __builtin_bit_cast(u32x4, c.Pk[QB][KS]);
  w[P] = cvtpk(c.S[X][KS >> 1][QB][8 * (KS & 1) + 2 * P], c.S[X][KS >> 1][QB][8 * (KS & 1) + 2 * P + 1]);
  c.Pk[QB][KS] = __builtin_bit_cast(bf16x8, w);
}
template <int X, int J> DI void rowsum1(W4C& c) {
  constexpr int qb = J >> 4, ks = (J >> 2) & 3, p = J & 3;
  c.rs0[qb] = fadd_s(c.rs0[qb], c.S[X][ks >> 1][qb][8 * (ks & 1) + 2 * p]); c.rs1[qb] = fadd_s(c.rs1[qb], c.S[X][ks >> 1][qb][8 * (ks & 1) + 2 * p + 1]);
}
template <int X, bool FIRST, int M> DI void gapA(W4C& c, unsigned vslot, const int t) {
  constexpr int Xp = X ^ 1;
  qk_mfma<X, FIRST, M>(c);
  if constexpr (M >= 16 && M <= 31) {
    constexpr int q = (M - 16) >> 1;
    if constexpr (((M - 16) & 1) == 0) {
      if constexpr (q < 4) c.pm0[q] = c.m0k + (unsigned)((t + 2) & 1) * 0x4000u + 1024u * q; else c.pm0[q] = c.m0v + (unsigned)((t + 1) & 1) * 0x4000u + 1024u * (q - 4);
      asm volatile("" : "+s"(c.pm0[q]));
    } else {
      if constexpr (q < 4) c.psf[q] = KTILEB * (unsigned)(t + 2) + c.sofk + KRS * 4u * q; else c.psf[q] = KTILEB * (unsigned)(t + 1) + c.sofv + KRS * 8u * ((q - 4) >> 1) + 128u * ((q - 4) & 1);
      asm volatile("" : "+s"(c.psf[q]));
    }
  }
  if constexpr (M == 31) asm volatile("s_nop 4" : "+s"(c.pm0[0]), "+s"(c.pm0[1]), "+s"(c.pm0[2]), "+s"(c.pm0[3]), "+s"(c.pm0[4]), "+s"(c.pm0[5]), "+s"(c.pm0[6]), "+s"(c.pm0[7]),
                                                  "+s"(c.psf[0]), "+s"(c.psf[1]), "+s"(c.psf[2]), "+s"(c.psf[3]), "+s"(c.psf[4]), "+s"(c.psf[5]), "+s"(c.psf[6]), "+s"(c.psf[7]));
  if constexpr (!FIRST && M == 8) asm volatile("" :: "v"(c.NEGM[0]), "v"(c.NEGM[1]));
  if constexpr (!FIRST) {
    constexpr int base = M + (M >= 4) + (M >= 8) + (M >= 12) + (M >= 16) + (M >= 20) + (M >= 24);
    if constexpr (M <= 25) tr_read<base>(c, vslot);
    if constexpr (M == 3 || M == 7 || M == 11 || M == 15 || M == 19 || M == 23) tr_read<base + 1>(c, vslot);
    if constexpr (M < 16) {
      constexpr int e0 = 2 * M, e1 = 2 * M + 1;
      c.S[Xp][1][e0 >> 4][e0 & 15] = __builtin_amdgcn_exp2f(c.S[Xp][1][e0 >> 4][e0 & 15]);
      c.S[Xp][1][e1 >> 4][e1 & 15] = __builtin_amdgcn_exp2f(c.S[Xp][1][e1 >> 4][e1 & 15]);
      pack1<Xp, (M >> 3), ((M >> 2) & 1), (M & 3)>(c);
    } else {
      constexpr int cc = M - 16;
      pack1<Xp, (cc >> 3), 2 + ((cc >> 2) & 1), (cc & 3)>(c);
      rowsum1<Xp, 16 * (cc >> 3) + 8 + 4 * ((cc >> 2) & 1) + (cc & 3)>(c);
    }
  }
  GAP();
}
#define W4_VLO "+v"(c.vlo[0][0]), "+v"(c.vlo[0][1]), "+v"(c.vlo[0][2]), "+v"(c.vlo[0][3]), "+v"(c.vlo[1][0]), "+v"(c.vlo[1][1]), "+v"(c.vlo[1][2]), "+v"(c.vlo[1][3]), \
               "+v"(c.vlo[2][0]), "+v"(c.vlo[2][1]), "+v"(c.vlo[2][2]), "+v"(c.vlo[2][3]), "+v"(c.vlo[3][0]), "+v"(c.vlo[3][1]), "+v"(c.vlo[3][2]), "+v"(c.vlo[3][3])
#define W4_VUP "+v"(c.vup[0][0]), "+v"(c.vup[0][1]), "+v"(c.vup[0][2]), "+v"(c.vup[0][3]), "+v"(c.vup[1][0]), "+v"(c.vup[1][1]), "+v"(c.vup[1][2]), "+v"(c.vup[1][3]), \
               "+v"(c.vup[2][0]), "+v"(c.vup[2][1]), "+v"(c.vup[2][2]), "+v"(c.vup[2][3]), "+v"(c.vup[3][0]), "+v"(c.vup[3][1]), "+v"(c.vup[3][2]), "+v"(c.vup[3][3])
DI void v_concat(W4C& c) {
#pragma unroll
  for (int ks = 0; ks < 4; ++ks)
#pragma unroll
    for (int db = 0; db < 4; ++db) c.Vf[db][ks] = __builtin_shufflevector(c.vlo[db][ks], c.vup[db][ks], 0, 1, 2, 3, 4, 5, 6, 7);
}
DI void lsum_merge(W4C& c) {
#pragma unroll
  for (int qb = 0; qb < 2; ++qb) {
    const float rs = fadd_s(c.rs0[qb], c.rs1[qb]);
    auto pr = __builtin_amdgcn_permlane32_swap(__float_as_uint(rs), __float_as_uint(rs), false, false);
    c.lsum[qb] = fadd_s(c.lsum[qb], fadd_s(__uint_as_float(pr[0]), __uint_as_float(pr[1])));
  }
}
template <int X, bool FIRST, int... Ms> DI void phaseA_(W4C& c, const int t, iseq<Ms...>) {
  const unsigned vslot = (unsigned)((t - 1) & 1) * 0x4000u;
  (gapA<X, FIRST, Ms>(c, vslot, t), ...);
  if constexpr (!FIRST) lsum_merge(c);
  if constexpr (FIRST) asm volatile("s_waitcnt vmcnt(0) lgkmcnt(0)\n\ts_barrier" ::: "memory");
  else { asm volatile("s_waitcnt vmcnt(0) lgkmcnt(0)" : W4_VLO :: "memory"); asm volatile("s_barrier" : W4_VUP :: "memory"); }
  GAP();
  if constexpr (!FIRST) v_concat(c);
}
template <int X, bool FIRST> DI void phaseA(W4C& c, const int t) { phaseA_<X, FIRST>(c, t, make_iseq<32>{}); }

template <int X, bool PEEL, bool ZERO1, bool DK, bool DV, bool KR, int M> DI void gapB(W4C& c, const unsigned kslot_rd) {
  if constexpr (!PEEL) pv_mfma<M, ZERO1>(c);
  if constexpr (M < 4 && DK) dma16(c.pm0[M], c.vksrc[M & 1], c.srdK, c.psf[M]);
  else if constexpr (M >= 4 && M < 8 && DV) dma16(c.pm0[M], c.vvsrc, c.srdV, c.psf[M]);
  if constexpr (M >= 1 && M <= 16 && KR) k_read<M - 1>(c, kslot_rd);
  if constexpr (M >= 8 && M < 16) {
    constexpr int i = M - 8, q = i >> 2, r = 4 * (i & 3);
    if constexpr ((i & 3) == 0) { c.pmx0[q] = max3f(c.S[X][0][q][0], c.S[X][0][q][1], c.S[X][1][q][0]); c.pmx1[q] = max3f(c.S[X][0][q][2], c.S[X][0][q][3], c.S[X][1][q][1]);
                                  c.pmx0[q] = max3f(c.pmx0[q], c.S[X][1][q][2], c.S[X][1][q][3]); }
    else                        { c.pmx0[q] = max3f(c.pmx0[q], c.S[X][0][q][r], c.S[X][0][q][r + 1]); c.pmx1[q] = max3f(c.pmx1[q], c.S[X][0][q][r + 2], c.S[X][0][q][r + 3]);
                                  c.pmx0[q] = max3f(c.pmx0[q], c.S[X][1][q][r], c.S[X][1][q][r + 1]); c.pmx1[q] = max3f(c.pmx1[q], c.S[X][1][q][r + 2], c.S[X][1][q][r + 3]); }
  }
  if constexpr (M == 16 || M == 17) {
    constexpr int q = M - 16; const float mm = max2f(c.pmx0[q], c.pmx1[q]);
    auto pr = __builtin_amdgcn_permlane32_swap(__float_as_uint(mm), __float_as_uint(mm), false, false);
    c.rmax[q] = max2f(__uint_as_float(pr[0]), __uint_as_float(pr[1]));
  }
  if constexpr (M == 18) {
    if constexpr (PEEL) {
#pragma unroll
      for (int q = 0; q < 2; ++q) { c.mhat[q] = c.rmax[q]; c.lsum[q] = 0.f; c.fct[q] = 1.f;
#pragma unroll
        for (int kb = 0; kb < 2; ++kb) { _Pragma("unroll") for (int e = 0; e < 16; ++e) c.S[X][kb][q][e] = fsub_s(c.S[X][kb][q][e], c.rmax[q]); }
        negm_bcast(c.NEGM[q], w4_aone(), -c.mhat[q]);
      }
    } else {
      const bool grew = (c.rmax[0] > THR) | (c.rmax[1] > THR);
      if (__builtin_expect(__any((int)grew), 0)) {
#pragma unroll
        for (int q = 0; q < 2; ++q) { const float dl = max2f(c.rmax[q], 0.f); c.delta[q] = dl; c.mhat[q] = fadd_s(c.mhat[q], dl); c.fct[q] = __builtin_amdgcn_exp2f(-dl);
#pragma unroll
          for (int kb = 0; kb < 2; ++kb) { _Pragma("unroll") for (int e = 0; e < 16; ++e) c.S[X][kb][q][e] = fsub_s(c.S[X][kb][q][e], dl); }
          negm_bcast(c.NEGM[q], w4_aone(), -c.mhat[q]);
        }
        c.pend = true;
      }
    }
  }
  if constexpr (M == 18) { c.rs0[0] = c.rs0[1] = c.rs1[0] = c.rs1[1] = 0.f; }
  if constexpr (M >= 19) {
    constexpr int g = M - 19, e_lo = (32 * g) / 13, e_hi = (32 * (g + 1)) / 13;
    {
      constexpr int p_lo = g ? (32 * (g - 1)) / 13 : 0, p_hi = g ? (32 * g) / 13 : 0;
      if constexpr (p_lo + 0 < p_hi) { constexpr int e = p_lo + 0; if constexpr (e & 1) c.rs1[e >> 4] = fadd_s(c.rs1[e >> 4], c.S[X][0][e >> 4][e & 15]); else c.rs0[e >> 4] = fadd_s(c.rs0[e >> 4], c.S[X][0][e >> 4][e & 15]); }
      if constexpr (p_lo + 1 < p_hi) { constexpr int e = p_lo + 1; if constexpr (e & 1) c.rs1[e >> 4] = fadd_s(c.rs1[e >> 4], c.S[X][0][e >> 4][e & 15]); else c.rs0[e >> 4] = fadd_s(c.rs0[e >> 4], c.S[X][0][e >> 4][e & 15]); }
      if constexpr (p_lo + 2 < p_hi) { constexpr int e = p_lo + 2; if constexpr (e & 1) c.rs1[e >> 4] = fadd_s(c.rs1[e >> 4], c.S[X][0][e >> 4][e & 15]); else c.rs0[e >> 4] = fadd_s(c.rs0[e >> 4], c.S[X][0][e >> 4][e & 15]); }
    }
    if constexpr (e_lo + 0 < e_hi) c.S[X][0][(e_lo + 0) >> 4][(e_lo + 0) & 15] = __builtin_amdgcn_exp2f(c.S[X][0][(e_lo + 0) >> 4][(e_lo + 0) & 15]);
    if constexpr (e_lo + 1 < e_hi) c.S[X][0][(e_lo + 1) >> 4][(e_lo + 1) & 15] = __builtin_amdgcn_exp2f(c.S[X][0][(e_lo + 1) >> 4][(e_lo + 1) & 15]);
    if constexpr (e_lo + 2 < e_hi) c.S[X][0][(e_lo + 2) >> 4][(e_lo + 2) & 15] = __builtin_amdgcn_exp2f(c.S[X][0][(e_lo + 2) >> 4][(e_lo + 2) & 15]);
  }
  if constexpr (M == 31) asm volatile("" :: "v"(c.S[X][0][0]), "v"(c.S[X][0][1]), "v"(c.NEGM[0]), "v"(c.NEGM[1]), "v"(c.rs0[0]), "v"(c.rs0[1]), "v"(c.rs1[0]), "v"(c.rs1[1]));
  GAP();
}
template <int R> DI void o_scale1(float f0, float f1) {
  float t; asm volatile("v_accvgpr_read_b32 %0, a[%c2]\n\tv_mul_f32 %0, %1, %0\n\tv_accvgpr_write_b32 a[%c2], %0" : "=&v"(t) : "v"(((R >> 4) & 1) ? f1 : f0), "i"(R) : ACLOB_LO);
}
template <int... Rs> DI void o_scale(float f0, float f1, iseq<Rs...>) { (o_scale1<Rs>(f0, f1), ...); }
template <int X, bool PEEL, bool ZERO1, bool DK, bool DV, bool KR, int... Ms> DI void phaseB_(W4C& c, const int t, iseq<Ms...>) {
  const unsigned kslot_rd = (unsigned)((t + 1) & 1) * 0x4000u;
  if constexpr (!PEEL) asm volatile("s_nop 1" ::: "memory");
  GAP();
  (gapB<X, PEEL, ZERO1, DK, DV, KR, Ms>(c, kslot_rd), ...);
#pragma unroll
  for (int e = (32 * 12) / 13; e < 32; ++e) { if (e & 1) c.rs1[e >> 4] = fadd_s(c.rs1[e >> 4], c.S[X][0][e >> 4][e & 15]); else c.rs0[e >> 4] = fadd_s(c.rs0[e >> 4], c.S[X][0][e >> 4][e & 15]); }
  if constexpr (!PEEL) {
    if (__builtin_expect(c.pend, 0)) {
      asm volatile("s_nop 7\n\ts_nop 7" ::: "memory");
      o_scale(c.fct[0], c.fct[1], make_iseq<128>{});
      asm volatile("s_nop 1" ::: "memory");
      c.lsum[0] = fmul_s(c.lsum[0], c.fct[0]); c.lsum[1] = fmul_s(c.lsum[1], c.fct[1]); c.pend = false;
    }
  }
  asm volatile("s_waitcnt lgkmcnt(0)" ::: "memory");
  GAP();
}
template <int X, bool PEEL, bool ZERO1, bool DK = true, bool DV = true, bool KR = true> DI void phaseB(W4C& c, const int t) { phaseB_<X, PEEL, ZERO1, DK, DV, KR>(c, t, make_iseq<32>{}); }
template <int... Is> DI void tr_reads(W4C& c, unsigned vslot, iseq<Is...>) { (tr_read<Is>(c, vslot), ...); }
template <int X, int... Cs> DI void packs(W4C& c, iseq<Cs...>) { (pack1<X, ((Cs >> 3) & 1), 2 * (Cs >> 4) + ((Cs >> 2) & 1), (Cs & 3)>(c), ...); }
template <int X, int J> DI void rowsum1_padded(W4C& c) { rowsum1<X, J>(c); asm volatile("s_nop 1"); }
template <int X, int... Js> DI void rowsums(W4C& c, iseq<Js...>) { (rowsum1_padded<X, Js>(c), ...); }
template <int... Ms> DI void pv_all(W4C& c, iseq<Ms...>) { (pv_mfma<Ms, false>(c), ...); }

struct QPC { unsigned vtr, vwsrc, m0w, sofw, vx; rsrc_t srdW, srdX; bf16x4 wl[2][4][4], wu[2][4][4]; };
template <int PAR, int I> DI void qp_tr(QPC& q, unsigned vaddr) {
  constexpr int ks = I >> 3, db = (I >> 1) & 3;
  if constexpr ((I & 1) == 0) asm volatile("ds_read_b64_tr_b16 %0, %1 offset:%2" : "=v"(q.wl[PAR][db][ks]) : "v"(vaddr), "i"(4096 * ks + 512 * db) : "memory");
  else                        asm volatile("ds_read_b64_tr_b16 %0, %1 offset:%2" : "=v"(q.wu[PAR][db][ks]) : "v"(vaddr), "i"(4096 * ks + 256 + 512 * db) : "memory");
}
template <int T, int Q> DI void qp_w(QPC& q) { dma16(q.m0w + (T % 6) * 0x4000u + 1024u * Q, q.vwsrc, q.srdW, q.sofw + T * 64u * 8192u + 8192u * 8u * (Q >> 1) + 128u * (Q & 1)); }
template <int T, int J> DI void qp_x(QPC& q) {
  constexpr int a = 128 + 32 * (T & 3) + 4 * J;
  asm volatile("buffer_load_dwordx4 a[%c3:%c4], %0, %1, %2 offen offset:%c5" :: "v"(q.vx), "s"(q.srdX), "s"((unsigned)((J >> 2) * 40 * 1024 + T * 4096)), "i"(a), "i"(a + 3), "i"((J & 3) * 1024) : "memory", ACLOB_HI);
}
template <int T> DI void qp_wt(QPC& q) { qp_w<T, 0>(q); qp_w<T, 1>(q); qp_w<T, 2>(q); qp_w<T, 3>(q); }
template <int T> DI void qp_xt(QPC& q) { qp_x<T, 0>(q); qp_x<T, 1>(q); qp_x<T, 2>(q); qp_x<T, 3>(q); qp_x<T, 4>(q); qp_x<T, 5>(q); qp_x<T, 6>(q); qp_x<T, 7>(q); }
#define QP_L(P) "+v"(q.wl[P][0][0]), "+v"(q.wl[P][0][1]), "+v"(q.wl[P][0][2]), "+v"(q.wl[P][0][3]), "+v"(q.wl[P][1][0]), "+v"(q.wl[P][1][1]), "+v"(q.wl[P][1][2]), "+v"(q.wl[P][1][3]), \
                "+v"(q.wl[P][2][0]), "+v"(q.wl[P][2][1]), "+v"(q.wl[P][2][2]), "+v"(q.wl[P][2][3]), "+v"(q.wl[P][3][0]), "+v"(q.wl[P][3][1]), "+v"(q.wl[P][3][2]), "+v"(q.wl[P][3][3])
#define QP_U(P) "+v"(q.wu[P][0][0]), "+v"(q.wu[P][0][1]), "+v"(q.wu[P][0][2]), "+v"(q.wu[P][0][3]), "+v"(q.wu[P][1][0]), "+v"(q.wu[P][1][1]), "+v"(q.wu[P][1][2]), "+v"(q.wu[P][1][3]), \
                "+v"(q.wu[P][2][0]), "+v"(q.wu[P][2][1]), "+v"(q.wu[P][2][2]), "+v"(q.wu[P][2][3]), "+v"(q.wu[P][3][0]), "+v"(q.wu[P][3][1]), "+v"(q.wu[P][3][2]), "+v"(q.wu[P][3][3])
template <int T, int M> DI void qp_gap(QPC& q, W4C& c, unsigned vrd) {
  constexpr int ks = M >> 3, db = (M >> 1) & 3, qb = M & 1, ao = AO_(db, qb), ax = 128 + 32 * (T & 3) + 16 * qb + 4 * ks, PAR = T & 1;
  const bf16x8 wf = __builtin_shufflevector(q.wl[PAR][db][ks], q.wu[PAR][db][ks], 0, 1, 2, 3, 4, 5, 6, 7);
  if constexpr (T == 0 && ks == 0) asm volatile("s_nop 1\n\tv_mfma_f32_32x32x16_bf16 a[%c1:%c2], %0, a[%c3:%c4], 0" :: "v"(wf), "i"(ao), "i"(ao + 15), "i"(ax), "i"(ax + 3) : ACLOB);
  else                           asm volatile("v_mfma_f32_32x32x16_bf16 a[%c1:%c2], %0, a[%c3:%c4], a[%c1:%c2]" :: "v"(wf), "i"(ao), "i"(ao + 15), "i"(ax), "i"(ax + 3) : ACLOB);
  if constexpr (T < 9 && M < 16) { qp_tr<PAR ^ 1, 2 * M>(q, vrd); qp_tr<PAR ^ 1, 2 * M + 1>(q, vrd); }
  if constexpr (T <= 3 && M >= 16 && M < 20) qp_w<T + 6, M - 16>(q);
  if constexpr (T >= 1 && T <= 6 && M >= 20 && M < 28) qp_x<T + 3, M - 20>(q);
  if constexpr (T == 4 && M >= 28) dma16(c.m0k + 1024u * (M - 28), c.vksrc[M & 1], c.srdK, c.sofk + KRS * 4u * (M - 28));
  if constexpr (T == 5 && M >= 16 && M < 20) dma16(c.m0v + 1024u * (M - 16), c.vvsrc, c.srdV, c.sofv + KRS * 8u * ((M - 16) >> 1) + 128u * (M & 1));
  if constexpr (T == 5 && M >= 28) dma16(c.m0k + 0x4000u + 1024u * (M - 28), c.vksrc[M & 1], c.srdK, KTILEB + c.sofk + KRS * 4u * (M - 28));
  if constexpr (T == 9 && M >= 1 && M <= 16) k_read<M - 1>(c, 0u);
  GAP();
}
template <int T, int VM, int... Ms> DI void qp_step_(QPC& q, W4C& c, iseq<Ms...>) {
  const unsigned vrd = q.vtr + ((T + 1) % 6) * 0x4000u;
  if constexpr (T & 1) { asm volatile("s_waitcnt vmcnt(%c16) lgkmcnt(0)" : QP_L(1) : "i"(VM) : "memory"); asm volatile("s_barrier" : QP_U(1) :: "memory"); }
  else                 { asm volatile("s_waitcnt vmcnt(%c16) lgkmcnt(0)" : QP_L(0) : "i"(VM) : "memory"); asm volatile("s_barrier" : QP_U(0) :: "memory"); }
  GAP();
  (qp_gap<T, Ms>(q, c, vrd), ...);
}
template <int T, int VM> DI void qp_step(QPC& q, W4C& c) { qp_step_<T, VM>(q, c, make_iseq<32>{}); }
template <int... Is> DI void qp_tr0(QPC& q, iseq<Is...>) { (qp_tr<0, Is>(q, q.vtr), ...); }
template <int R> DI u32x4 acc_pack8(float f) {
  float o[8];
  asm volatile("v_accvgpr_read_b32 %0, a[%c8]\n\tv_accvgpr_read_b32 %1, a[%c9]\n\tv_accvgpr_read_b32 %2, a[%c10]\n\tv_accvgpr_read_b32 %3, a[%c11]\n\t"
               "v_accvgpr_read_b32 %4, a[%c12]\n\tv_accvgpr_read_b32 %5, a[%c13]\n\tv_accvgpr_read_b32 %6, a[%c14]\n\tv_accvgpr_read_b32 %7, a[%c15]"
               : "=v"(o[0]), "=v"(o[1]), "=v"(o[2]), "=v"(o[3]), "=v"(o[4]), "=v"(o[5]), "=v"(o[6]), "=v"(o[7])
               : "i"(R), "i"(R + 1), "i"(R + 2), "i"(R + 3), "i"(R + 4), "i"(R + 5), "i"(R + 6), "i"(R + 7));
  u32x4 w;
#pragma unroll
  for (int e = 0; e < 4; ++e) w[e] = cvtpk(o[2 * e] * f, o[2 * e + 1] * f);
  return w;
}
template <int I> DI void q_conv1(float C) {
  constexpr int qb = I >> 3, d0 = I & 7, aq = AQ_(qb, d0);
  const u32x4 w = acc_pack8<AO_(d0 >> 1, qb) + 8 * (d0 & 1)>(C);
  asm volatile("v_accvgpr_write_b32 a[%c4], %0\n\tv_accvgpr_write_b32 a[%c5], %1\n\tv_accvgpr_write_b32 a[%c6], %2\n\tv_accvgpr_write_b32 a[%c7], %3"
               :: "v"(w[0]), "v"(w[1]), "v"(w[2]), "v"(w[3]), "i"(aq), "i"(aq + 1), "i"(aq + 2), "i"(aq + 3) : ACLOB_HI);
}
template <int... Is> DI void q_conv(float C, iseq<Is...>) { (q_conv1<Is>(C), ...); }
template <int I> DI void o_store1(unsigned char* dst, float il0, float il1) {
  constexpr int qb = I >> 3, j = I & 7;
  const u32x4 w = acc_pack8<AO_(j >> 1, qb) + 8 * (j & 1)>(qb ? il1 : il0);
  *(u32x4*)(dst + (size_t)qb * 256u * 1024u + j * 1024u) = w;
}
template <int... Is> DI void o_store(unsigned char* dst, float il0, float il1, iseq<Is...>) { (o_store1<Is>(dst, il0, il1), ...); }
DI void gload16(u32x4& r, const void* p) { asm volatile("global_load_dwordx4 %0, %1, off" : "=v"(r) : "v"(p) : "memory"); }
DI void gstore16(void* p, const u32x4& r) { asm volatile("global_store_dwordx4 %0, %1, off\n\ts_nop 1" :: "v"(p), "v"(r) : "memory"); }

#define TS(i)
__global__ __launch_bounds__(256, 1)
void k_attn(const unsigned char* __restrict__ xnF, const unsigned short* __restrict__ wq, const unsigned short* __restrict__ Kc, const unsigned short* __restrict__ Vc,
            unsigned char* __restrict__ OF, const unsigned char* __restrict__ zF, unsigned char* __restrict__ z2nT,
            const unsigned short* __restrict__ w_sp, unsigned char* __restrict__ wspF) {
  extern __shared__ __attribute__((aligned(16))) char lds[];
  const unsigned tid = threadIdx.x, lane = tid & 63u, wid = rfl(tid >> 6), l32 = lane & 31u, hi = lane >> 5;
  constexpr int T = NTILE;
  const float C = 0.12751743082f;
  const unsigned xcd = blockIdx.x & 7u, idx = blockIdx.x >> 3;
  const unsigned h = 8u * (xcd >> 1) + (idx & 7u), y = 4u * (xcd & 1u) + (idx >> 3);
  const unsigned rb0 = 8u * y + 2u * wid;
  W4C c; QPC q;
  TS(0);
  c.ldsb = (unsigned)(size_t)lds;
#pragma unroll
  for (int j = 0; j < 4; ++j) c.vkb[j] = c.ldsb + KRING + 256u * l32 + 16u * ((2u * j + hi) ^ (l32 & 7u));
  c.vvb = c.ldsb + VRING + tr_lane<false, false>(lane, 0u);
#pragma unroll
  for (int par = 0; par < 2; ++par) c.vksrc[par] = KRS * (lane >> 4) + 16u * ((lane & 15u) ^ (4u * par + (lane >> 4)));
  c.vvsrc = KRS * ((lane >> 2) & 7u) + 64u * hi + 16u * (lane & 3u);
  c.m0k = rfl(c.ldsb + KRING + 4096u * wid); c.m0v = rfl(c.ldsb + VRING + 4096u * wid);
  c.sofk = KRS * 16u * wid; c.sofv = KRS * 16u * wid;
  c.srdK = mk_srd((const char*)Kc + (size_t)h * SEQ * 256u, SEQ * 256u);
  c.srdV = mk_srd((const char*)Vc + (size_t)h * SEQ * 256u, SEQ * 256u);
  q.vtr = c.ldsb + WRING + tr_lane<true, true>(lane, 2048u);
  q.vwsrc = 8192u * ((lane >> 2) & 7u) + 64u * hi + 16u * (lane & 3u);
  q.m0w = rfl(c.ldsb + WRING + 4096u * wid); q.sofw = 8192u * 16u * wid;
  q.vx = rb0 * 40u * 1024u + lane * 16u;
  q.srdW = mk_srd((const char*)wq + h * 256u, DM * 8192u - h * 256u);
  q.srdX = mk_srd(xnF, SEQ * DM * 2u);
  const unsigned srow = blockIdx.x * 8u + 2u * wid + hi;
  u32x4 zv[4], wv[8];
#pragma unroll
  for (int i = 0; i < 4; ++i) { const unsigned pc = l32 + 32u * i, p = pc < 108u ? pc : 107u;
    gload16(zv[i], zF + ((size_t)((srow >> 5) * 108u + 54u + (p >> 1)) * 64u + (p & 1u) * 32u + (srow & 31u)) * 16u); }
#pragma unroll
  for (int i = 0; i < 8; ++i) gload16(wv[i], (const char*)w_sp + (size_t)srow * 4096u + (l32 + 32u * i) * 16u);
  asm volatile("s_nop 4" ::: "memory");
  qp_wt<0>(q); qp_xt<0>(q); qp_wt<1>(q); qp_xt<1>(q); qp_wt<2>(q); qp_xt<2>(q); qp_wt<3>(q); qp_xt<3>(q);
  asm volatile("s_waitcnt vmcnt(48)" : "+v"(zv[0]), "+v"(zv[1]), "+v"(zv[2]), "+v"(zv[3]), "+v"(wv[0]), "+v"(wv[1]), "+v"(wv[2]), "+v"(wv[3]), "+v"(wv[4]), "+v"(wv[5]), "+v"(wv[6]), "+v"(wv[7]) :: "memory");
#pragma unroll
  for (int i = 0; i < 8; ++i) { const unsigned ch = l32 + 32u * i;
    gstore16(wspF + ((size_t)((srow >> 5) * 128u + (ch >> 1)) * 64u + (ch & 1u) * 32u + (srow & 31u)) * 16u, wv[i]); }
  {
    const bool v3 = l32 < 12u;
    float f[32], s = 0.f;
#pragma unroll
    for (int i = 0; i < 4; ++i)
#pragma unroll
      for (int e = 0; e < 4; ++e) { f[8 * i + 2 * e] = bflo(zv[i][e]); f[8 * i + 2 * e + 1] = bfhi(zv[i][e]); const float t2 = f[8 * i + 2 * e] + f[8 * i + 2 * e + 1]; s += (i < 3 || v3) ? t2 : 0.f; }
#pragma unroll
    for (int o = 16; o; o >>= 1) s += __shfl_xor(s, o);
    const float mean = s * (1.f / FH); float qq = 0.f;
#pragma unroll
    for (int i = 0; i < 32; ++i) { f[i] -= mean; qq += (i < 24 || v3) ? f[i] * f[i] : 0.f; }
#pragma unroll
    for (int o = 16; o; o >>= 1) qq += __shfl_xor(qq, o);
    const float rstd = 1.f / sqrtf(qq * (1.f / FH) + 1e-5f);
#pragma unroll
    for (int i = 0; i < 4; ++i) { const unsigned pc = l32 + 32u * i, p = pc < 108u ? pc : 107u;
      u32x4 w;
#pragma unroll
      for (int e = 0; e < 4; ++e) w[e] = cvtpk(f[8 * i + 2 * e] * rstd, f[8 * i + 2 * e + 1] * rstd);
      gstore16(z2nT + ((size_t)(blockIdx.x * 27u + (p >> 2)) * 8u + (srow & 7u)) * 64u + (p & 3u) * 16u, w); }
  }
  qp_wt<4>(q); qp_wt<5>(q);
  TS(4);
  wait_vm_bar<63>(); TS(5);
  qp_tr0(q, make_iseq<32>{});
  qp_step<0, 52>(q, c); qp_step<1, 44>(q, c); qp_step<2, 44>(q, c); qp_step<3, 32>(q, c); qp_step<4, 24>(q, c);
  qp_step<5, 24>(q, c); qp_step<6, 24>(q, c); qp_step<7, 24>(q, c); qp_step<8, 12>(q, c); qp_step<9, 0>(q, c);
  asm volatile("s_nop 7\n\ts_nop 7" ::: "memory");
  q_conv(C, make_iseq<16>{});
  asm volatile("s_waitcnt lgkmcnt(0)\n\ts_nop 1" ::: "memory");
  GAP();
  TS(1);
  c.mhat[0] = c.mhat[1] = 0.f; c.lsum[0] = c.lsum[1] = 0.f; c.fct[0] = c.fct[1] = 1.f; c.pend = false;
  phaseA<0, true>(c, 0);  phaseB<0, true, false>(c, 0);
  phaseA<1, false>(c, 1); phaseB<1, false, true>(c, 1);
  for (int t = 2; t < T - 2; t += 2) {
    phaseA<0, false>(c, t);     phaseB<0, false, false>(c, t);
    phaseA<1, false>(c, t + 1); phaseB<1, false, false>(c, t + 1);
  }
  TS(2);
  phaseA<0, false>(c, T - 2); phaseB<0, false, false, false, true, true>(c, T - 2);
  phaseA<1, false>(c, T - 1); phaseB<1, false, false, false, false, false>(c, T - 1);
  {
    const unsigned vslot = (unsigned)((T - 1) & 1) * 0x4000u;
    tr_reads(c, vslot, make_iseq<32>{});
#pragma unroll
    for (int e = 0; e < 32; ++e) c.S[1][1][e >> 4][e & 15] = __builtin_amdgcn_exp2f(c.S[1][1][e >> 4][e & 15]);
    packs<1>(c, make_iseq<32>{});
    rowsums<1>(c, iseq<8, 9, 10, 11, 12, 13, 14, 15, 24, 25, 26, 27, 28, 29, 30, 31>{});
    lsum_merge(c);
    asm volatile("s_waitcnt lgkmcnt(0)" : W4_VLO :: "memory"); asm volatile("" : W4_VUP :: "memory");
    GAP();
    v_concat(c);
    asm volatile("s_nop 1" ::: "memory");
    pv_all(c, make_iseq<32>{});
    asm volatile("s_nop 7\n\ts_nop 7" ::: "memory");
  }
  {
    unsigned lb; asm volatile("v_mbcnt_lo_u32_b32 %0, -1, 0\n\tv_mbcnt_hi_u32_b32 %0, -1, %0" : "=v"(lb));
    const float il0 = __builtin_amdgcn_rcpf(c.lsum[0]), il1 = __builtin_amdgcn_rcpf(c.lsum[1]);
    o_store(OF + ((size_t)(rb0 * 256u + 8u * h) * 64u + lb) * 16u, il0, il1, make_iseq<16>{});
  }
  TS(3);
}

extern "C" void kernel_launch(void* const* d_in, const int* in_sizes, int n_in, void* d_out, int out_size, void* d_ws, size_t ws_size, hipStream_t stream) {
  const unsigned short* x = (const unsigned short*)d_in[0];
  const unsigned short* Kc = (const unsigned short*)d_in[1];
  const unsigned short* Vc = (const unsigned short*)d_in[2];
  const unsigned short* wq = (const unsigned short*)d_in[3];
  const unsigned short* wo = (const unsigned short*)d_in[4];
  const unsigned short* w_in = (const unsigned short*)d_in[6];
  const unsigned short* w_sp = (const unsigned short*)d_in[7];
  const unsigned short* b_sp = (const unsigned short*)d_in[8];
  const unsigned short* w_out = (const unsigned short*)d_in[9];
  unsigned char* ws = (unsigned char*)d_ws;
  (void)hipFuncSetAttribute((const void*)k_attn, hipFuncAttributeMaxDynamicSharedMemorySize, 163840);
  (void)hipFuncSetAttribute((const void*)k_zgemm, hipFuncAttributeMaxDynamicSharedMemorySize, 163840);
  (void)hipFuncSetAttribute((const void*)k_mid, hipFuncAttributeMaxDynamicSharedMemorySize, 163840);
  (void)hipFuncSetAttribute((const void*)k_out, hipFuncAttributeMaxDynamicSharedMemorySize, 110592);
  hipLaunchKernelGGL(k_ln, dim3(256), dim3(256), 0, stream, x, ws + WS_XN);
  hipLaunchKernelGGL(k_zgemm, dim3(224), dim3(256), 163840, stream, ws + WS_XN, w_in, ws + WS_Z);
  hipLaunchKernelGGL(k_attn, dim3(256), dim3(256), 163840, stream, ws + WS_XN, wq, Kc, Vc, ws + WS_O, ws + WS_Z, ws + WS_Z2N, w_sp, ws + WS_WSP);
  hipLaunchKernelGGL(k_mid, dim3(256), dim3(256), 163840, stream, ws + WS_O, wo, ws + WS_SP, ws + WS_WSP, ws + WS_Z2N, ws + WS_Z, b_sp, ws + WS_U);
  hipLaunchKernelGGL(k_out, dim3(160), dim3(256), 110592, stream, ws + WS_U, w_out, ws + WS_SP, x, (unsigned short*)d_out);
}
```

```cpp
#include <hip/hip_runtime.h>
#include <hip/hip_bf16.h>
#include <utility>

typedef float    f32x16 __attribute__((ext_vector_type(16)));
typedef float    f32x2  __attribute__((ext_vector_type(2)));
typedef float    f32x4  __attribute__((ext_vector_type(4)));
typedef __bf16   bf16x8 __attribute__((ext_vector_type(8)));
typedef __bf16   bf16x4 __attribute__((ext_vector_type(4)));
typedef __bf16   bf16x2 __attribute__((ext_vector_type(2)));
typedef unsigned u32x4  __attribute__((ext_vector_type(4)));
typedef unsigned u32x2  __attribute__((ext_vector_type(2)));
template <int... Is> using iseq = std::integer_sequence<int, Is...>;
template <int N> using make_iseq = std::make_integer_sequence<int, N>;
typedef __amdgpu_buffer_rsrc_t rsrc_t;

#define DI __device__ __forceinline__
#define GAP() __builtin_amdgcn_sched_barrier(0)
#define SRD_WORD3 0x00020000
#define A10(x) "a" #x "0", "a" #x "1", "a" #x "2", "a" #x "3", "a" #x "4", "a" #x "5", "a" #x "6", "a" #x "7", "a" #x "8", "a" #x "9"
#define ACLOB_LO "a0", "a1", "a2", "a3", "a4", "a5", "a6", "a7", "a8", "a9", A10(1), A10(2), A10(3), A10(4), A10(5), A10(6), A10(7), A10(8), A10(9), A10(10), A10(11), \
                 "a120", "a121", "a122", "a123", "a124", "a125", "a126", "a127"
#define ACLOB_HI "a128", "a129", A10(13), A10(14), A10(15), A10(16), A10(17), A10(18), A10(19), A10(20), A10(21), A10(22), A10(23), A10(24), \
                 "a250", "a251", "a252", "a253", "a254", "a255"
#define ACLOB ACLOB_LO, ACLOB_HI

constexpr int SEQ = 2048, DM = 640, NH = 32, DH = 128, FF = 1728, FH = 864;
constexpr size_t WS_XN  = 0;
constexpr size_t WS_Z   = WS_XN  + (size_t)SEQ * DM * 2;
constexpr size_t WS_O   = WS_Z   + (size_t)SEQ * FF * 2;
constexpr size_t WS_Z2N = WS_O   + (size_t)SEQ * NH * DH * 2;
constexpr size_t WS_WSP = WS_Z2N + (size_t)SEQ * FH * 2;
constexpr size_t WS_SP  = WS_WSP + (size_t)SEQ * SEQ * 2;
constexpr size_t WS_U   = WS_SP  + (size_t)2 * SEQ * DM * 4;
constexpr size_t WS_END = WS_U   + (size_t)SEQ * FH * 2;

DI unsigned rfl(unsigned x) { return __builtin_amdgcn_readfirstlane(x); }
DI unsigned cvtpk(float lo, float hi) { f32x2 v = {lo, hi}; bf16x2 b = __builtin_convertvector(v, bf16x2); return __builtin_bit_cast(unsigned, b); }
DI float bflo(unsigned w) { return __uint_as_float(w << 16); }
DI float bfhi(unsigned w) { return __uint_as_float(w & 0xffff0000u); }
DI rsrc_t mk_srd(const void* p, unsigned bytes) { return __builtin_amdgcn_make_buffer_rsrc((void*)p, (short)0, (int)bytes, SRD_WORD3); }
DI float max3f(float a, float b, float c) { float r; asm("v_max3_f32 %0, %1, %2, %3" : "=v"(r) : "v"(a), "v"(b), "v"(c)); return r; }
DI float max2f(float a, float b) { float r; asm("v_max_f32_e32 %0, %1, %2" : "=v"(r) : "v"(a), "v"(b)); return r; }
DI float fadd_s(float a, float b) { float r; asm("v_add_f32_e32 %0, %1, %2" : "=v"(r) : "v"(a), "v"(b)); return r; }
DI float fmul_s(float a, float b) { float r; asm("v_mul_f32_e32 %0, %1, %2" : "=v"(r) : "v"(a), "v"(b)); return r; }
DI float fsub_s(float a, float b) { float r; asm("v_sub_f32_e32 %0, %1, %2" : "=v"(r) : "v"(a), "v"(b)); return r; }
template <int N> DI void wait_vm_bar() { asm volatile("s_waitcnt vmcnt(%c0) lgkmcnt(0)\n\ts_barrier" :: "i"(N > 63 ? 63 : N) : "memory"); }
DI void dma16(unsigned m0v, unsigned voff, rsrc_t srd, unsigned soff) {
  asm volatile("s_mov_b32 m0, %0\n\ts_nop 0\n\tbuffer_load_dwordx4 %1, %2, %3 offen lds" :: "s"(m0v), "v"(voff), "s"(srd), "s"(soff) : "m0", "memory");
}
DI void touch8k(const char* p) { unsigned d0, d1; asm volatile("global_load_dword %0, %2, off\n\tglobal_load_dword %1, %2, off offset:64" : "=&v"(d0), "=&v"(d1) : "v"(p) : "memory"); }
template <bool NATK, bool SWAPN> DI unsigned tr_lane(unsigned lane, unsigned halfstride) {
  const unsigned c = lane & 3u, cs = SWAPN ? (((c & 1u) << 1) | (c >> 1)) : c, hi = lane >> 5;
  return (cs << 3) | (((lane >> 2) & 3u) << 6) | (((lane >> 4) & 1u) << 5) | (NATK ? hi * halfstride : (hi << 8));
}

#define TS(k, i)
__global__ __launch_bounds__(256) void k_ln(const unsigned short* __restrict__ x, unsigned char* __restrict__ xnF, const unsigned short* __restrict__ w_in, const unsigned short* __restrict__ wq) {
  const unsigned lane = threadIdx.x & 63u, wid = threadIdx.x >> 6, l = lane & 31u;
  const unsigned row = blockIdx.x * 8u + wid * 2u + (lane >> 5);
  const u32x4* src = (const u32x4*)(x + (size_t)row * DM);
  u32x4 v[3]; v[0] = src[l]; v[1] = src[l + 32]; v[2] = (l < 16u) ? src[l + 64] : (u32x4){0u, 0u, 0u, 0u};
  float f[24]; float s = 0.f;
#pragma unroll
  for (int i = 0; i < 3; ++i)
#pragma unroll
    for (int e = 0; e < 4; ++e) { f[8 * i + 2 * e] = bflo(v[i][e]); f[8 * i + 2 * e + 1] = bfhi(v[i][e]); s += f[8 * i + 2 * e] + f[8 * i + 2 * e + 1]; }
#pragma unroll
  for (int o = 16; o; o >>= 1) s += __shfl_xor(s, o);
  const float mean = s * (1.f / DM); float q = 0.f;
#pragma unroll
  for (int i = 0; i < 24; ++i) { f[i] -= mean; if (i < 16 || l < 16u) q += f[i] * f[i]; }
#pragma unroll
  for (int o = 16; o; o >>= 1) q += __shfl_xor(q, o);
  const float rstd = 1.f / sqrtf(q * (1.f / DM) + 1e-5f);
#pragma unroll
  for (int i = 0; i < 3; ++i) {
    const unsigned c = l + 32u * i;
    if (i < 2 || l < 16u) {
      u32x4 w;
#pragma unroll
      for (int e = 0; e < 4; ++e) w[e] = cvtpk(f[8 * i + 2 * e] * rstd, f[8 * i + 2 * e + 1] * rstd);
      *(u32x4*)(xnF + ((size_t)((c >> 1) * 64u + (row >> 5)) * 64u + (c & 1u) * 32u + (row & 31u)) * 16u) = w;
    }
  }
  { const unsigned gw = blockIdx.x * 4u + wid;
    if (gw < 270u) touch8k((const char*)w_in + gw * 8192u + lane * 128u); else if (gw < 910u) touch8k((const char*)wq + (gw - 270u) * 8192u + lane * 128u); }
}

constexpr int LA = 6;
template <int NT, int KS> struct GemmC {
  static constexpr int P = NT * KS / 4, TILEB = NT * KS * 1024, G = P + KS, NM = NT * KS;
  unsigned vw[P];
  unsigned vx[KS];
  unsigned vtr;
  unsigned m0w;
  unsigned wstride;
  rsrc_t srdW, srdX;
  int kid;
  f32x16 acc[NT];
};
struct NoEx { static constexpr int N = 0; template <int J> DI void issue() {} };
template <int KS> constexpr int GX(int u, int ks) { return 32 + (u * KS + ks) * 4; }
template <int NT, int KS, bool NATK, int AS, int F> DI void g_tr(unsigned vaddr) {
  constexpr int ks = F / NT, nt = F % NT, a = 4 * AS;
  constexpr int o0 = NATK ? ((2 * ks) * NT + nt) * 512 : ((2 * ks) * NT + nt) * 512, o1 = NATK ? o0 + 256 : o0 + NT * 512;
  asm volatile("ds_read_b64_tr_b16 a[%c1:%c2], %0 offset:%c5\n\tds_read_b64_tr_b16 a[%c3:%c4], %0 offset:%c6" :: "v"(vaddr), "i"(a), "i"(a + 1), "i"(a + 2), "i"(a + 3), "i"(o0), "i"(o1) : "memory", ACLOB);
}
template <int NT, int KS, int XI, int KSI> DI void g_xload(GemmC<NT, KS>& c, unsigned soff) {
  constexpr int a = GX<KS>(XI, KSI);
  asm volatile("buffer_load_dwordx4 a[%c3:%c4], %0, %1, %2 offen" :: "v"(c.vx[KSI]), "s"(c.srdX), "s"(soff), "i"(a), "i"(a + 3) : "memory", ACLOB);
}
template <int NT, int KS, int AS, int XI, bool ZERO, int LG, int M> DI void g_mfma(GemmC<NT, KS>& c) {
  constexpr int ks = M / NT, nt = M % NT, a = 4 * AS, b = GX<KS>(XI, ks);
  if constexpr (ZERO && ks == 0) asm volatile("s_waitcnt lgkmcnt(%c5)\n\tv_mfma_f32_32x32x16_bf16 %0, a[%c1:%c2], a[%c3:%c4], 0" : "=v"(c.acc[nt]) : "i"(a), "i"(a + 3), "i"(b), "i"(b + 3), "i"(LG) : "memory", ACLOB);
  else asm volatile("s_waitcnt lgkmcnt(%c5)\n\tv_mfma_f32_32x32x16_bf16 %0, a[%c1:%c2], a[%c3:%c4], %0" : "+v"(c.acc[nt]) : "i"(a), "i"(a + 3), "i"(b), "i"(b + 3), "i"(LG) : "memory", ACLOB);
}
template <int NT, int KS, int D, int GS, int Q> DI void g_vmem(GemmC<NT, KS>& c, unsigned g) {
  constexpr int P = GemmC<NT, KS>::P, G = GemmC<NT, KS>::G, gi = Q / G, q = Q % G, slot = (GS + gi) % D;
  if constexpr (q < P) dma16(c.m0w + slot * GemmC<NT, KS>::TILEB + 1024u * q, c.vw[q], c.srdW, (g + gi) * c.wstride);
  else g_xload<NT, KS, slot, q - P>(c, (g + gi) * (KS * 65536u));
}
template <int NT, int KS, int D, int... Q> DI void g_pre(GemmC<NT, KS>& c, iseq<Q...>) { (g_vmem<NT, KS, D, 0, Q>(c, 0u), ...); }
template <int NT, int KS, bool NATK, int D, int TM, bool FIRST, bool LAST, int GS, int NG, bool EXS, class EX, int J>
DI void g_gap(GemmC<NT, KS>& c, EX& ex, unsigned vrd, unsigned g) {
  constexpr int NM = NT * KS, NOP = NG * GemmC<NT, KS>::G, OPG = (NOP + NM - 1) / NM, T1 = (TM + 1) % D;
  if constexpr (J < LA) { if constexpr (!FIRST) g_mfma<NT, KS, (TM * NM + NM - LA + J) & 7, TM, false, (LAST ? 2 * (LA - 1 - J) : 2 * (LA - 1)), NM - LA + J>(c); }
  else if constexpr (!LAST) g_mfma<NT, KS, (T1 * NM + J - LA) & 7, T1, FIRST, 2 * (LA - 1), J - LA>(c);
  if constexpr (!LAST) g_tr<NT, KS, NATK, (T1 * NM + J) & 7, J>(vrd);
  if constexpr (OPG * J < NOP) g_vmem<NT, KS, D, GS, OPG * J>(c, g);
  if constexpr (OPG == 2 && 2 * J + 1 < NOP) g_vmem<NT, KS, D, GS, 2 * J + 1>(c, g);
  if constexpr (EXS) { if constexpr (2 * J < EX::N) ex.template issue<2 * J>(); if constexpr (2 * J + 1 < EX::N) ex.template issue<2 * J + 1>(); }
  GAP();
}
template <int NT, int KS, bool NATK, int D, int TM, bool FIRST, bool LAST, int GS, int NG, bool EXS, int VM, class EX, int... Js>
DI void g_step_(GemmC<NT, KS>& c, EX& ex, unsigned g, iseq<Js...>) {
  const unsigned vrd = c.vtr + ((TM + 1) % D) * GemmC<NT, KS>::TILEB;
  if constexpr (!LAST) asm volatile("s_waitcnt vmcnt(%c0)\n\ts_barrier" :: "i"(VM > 63 ? 63 : VM) : "memory");
  if constexpr (FIRST) TS(c.kid, 1);
  GAP();
  (g_gap<NT, KS, NATK, D, TM, FIRST, LAST, GS, NG, EXS, EX, Js>(c, ex, vrd, g), ...);
}
constexpr int g_iss(int t, int NTILES, int PRE) {
  int i = 1;
  for (int u = -1; u <= t; ++u) { i += 2; if (i > u + 1 + PRE) i = u + 1 + PRE; if (i > NTILES - 1) i = NTILES - 1; }
  return i;
}
template <int NT, int KS, bool NATK, int NTILES, int D, int PRE, class EX, int T> DI void g_step_static(GemmC<NT, KS>& c, EX& ex) {
  constexpr int G = GemmC<NT, KS>::G, i0 = g_iss(T - 1, NTILES, PRE), i1 = g_iss(T, NTILES, PRE);
  constexpr bool done = (i0 == NTILES - 1), done_before = (g_iss(T - 2, NTILES, PRE) == NTILES - 1);
  constexpr int VM = (i0 - (T + 1)) * G + (done_before ? EX::N : 0);
  g_step_<NT, KS, NATK, D, (T + D) % D, (T == -1), (T == NTILES - 1), (i0 + 1) % D, i1 - i0, (done && !done_before), VM, EX>(c, ex, i0 + 1, make_iseq<(T == NTILES - 1) ? LA : NT * KS>{});
}
template <int NT, int KS, bool NATK, int NTILES, int D, int PRE, class EX, int T0, int... Ts> DI void g_tail(GemmC<NT, KS>& c, EX& ex, iseq<Ts...>) {
  (g_step_static<NT, KS, NATK, NTILES, D, PRE, EX, T0 + Ts>(c, ex), ...);
}
template <int NT, int KS, bool NATK, int D, int PRE, class EX, int... Ts> DI void g_body(GemmC<NT, KS>& c, EX& ex, int t, iseq<Ts...>) {
  (g_step_<NT, KS, NATK, D, Ts, false, false, (Ts + 1 + PRE) % D, 1, false, (PRE - 1) * GemmC<NT, KS>::G, EX>(c, ex, t + Ts + 1 + PRE, make_iseq<NT * KS>{}), ...);
}
template <int NT, int KS, bool NATK, int NTILES, int D, int PRE, class EX> DI void gemm_run(GemmC<NT, KS>& c, EX& ex) {
  static_assert(NTILES <= D || (PRE <= D - 2 && (D * NT * KS) % 8 == 0), "ring");
  static_assert(32 + D * KS * 4 <= 256 && 2 * NT * KS >= EX::N, "regs");
  TS(c.kid, 0);
  asm volatile("s_nop 4" ::: "memory");
  g_pre<NT, KS, D>(c, make_iseq<2 * GemmC<NT, KS>::G>{});
  if constexpr (NTILES <= D) g_tail<NT, KS, NATK, NTILES, D, PRE, EX, -1>(c, ex, make_iseq<NTILES + 1>{});
  else {
    constexpr int T0 = D * ((NTILES - 1 - PRE) / D);
    g_tail<NT, KS, NATK, NTILES, D, PRE, EX, -1>(c, ex, make_iseq<D + 1>{});
    for (int t = D; t < T0; t += D) g_body<NT, KS, NATK, D, PRE, EX>(c, ex, t, make_iseq<D>{});
    g_tail<NT, KS, NATK, NTILES, D, PRE, EX, T0>(c, ex, make_iseq<NTILES - T0>{});
  }
  asm volatile("s_waitcnt vmcnt(0)\n\ts_nop 7\n\ts_nop 7" ::: "memory");
#pragma unroll
  for (int nt = 0; nt < NT; ++nt) asm volatile("" : "+v"(c.acc[nt]));
  TS(c.kid, 2);
  GAP();
}
template <int NT, int KS> DI void set_vx(GemmC<NT, KS>& c, unsigned v) {
#pragma unroll
  for (int k = 0; k < KS; ++k) c.vx[k] = v + k * 65536u;
}
DI void bload16(u32x4& r, unsigned voff, rsrc_t srd, unsigned soff) { asm volatile("buffer_load_dwordx4 %0, %1, %2, %3 offen" : "=v"(r) : "v"(voff), "s"(srd), "s"(soff) : "memory"); }
template <int NT, int KS, bool NATK, bool SWAPN> DI void gemm_setup_rm(GemmC<NT, KS>& c, unsigned ldsb, unsigned lane, unsigned wid, unsigned ldB) {
  constexpr int P = GemmC<NT, KS>::P;
#pragma unroll
  for (int q = 0; q < P; ++q) { const unsigned p = wid * P + q, kg = p / (NT / 2), cp = p % (NT / 2);
    c.vw[q] = ldB * (8u * kg + ((lane >> 2) & 7u)) + 128u * cp + 64u * (lane >> 5) + 16u * (lane & 3u); }
  c.wstride = ldB * 16u * KS;
  c.m0w = rfl(ldsb + 1024u * P * wid);
  c.vtr = ldsb + tr_lane<NATK, SWAPN>(lane, NT * 512u);
}

DI float gelu_tanh(float v) {
  const float y2 = v * (-2.3022081985f - 0.1029432397f * v * v);
  return v * __builtin_amdgcn_rcpf(1.f + __builtin_amdgcn_exp2f(y2));
}
__global__ __launch_bounds__(256, 1) void k_zgemm(const unsigned char* __restrict__ xnF, const unsigned short* __restrict__ w_in, unsigned char* __restrict__ zF) {
  extern __shared__ __attribute__((aligned(16))) char lds[];
  const unsigned lane = threadIdx.x & 63u, wid = rfl(threadIdx.x >> 6);
  const unsigned xcd = blockIdx.x & 7u, idx = blockIdx.x >> 3;
  const unsigned rb128 = 4u * (xcd >> 1) + idx / 7u, cb = 7u * (xcd & 1u) + idx % 7u, rb = 4u * rb128 + wid;
  GemmC<4, 5> c; c.kid = 1;
  gemm_setup_rm<4, 5, true, false>(c, (unsigned)(size_t)lds, lane, wid, FF * 2u);
  c.srdW = mk_srd((const char*)w_in + cb * 256u, DM * FF * 2u - cb * 256u);
  c.srdX = mk_srd(xnF, SEQ * DM * 2u);
  set_vx(c, rb * 1024u + lane * 16u);
  NoEx ex; gemm_run<4, 5, true, 8, 8, 6>(c, ex);
#pragma unroll
  for (int nt = 0; nt < 4; ++nt) {
    const unsigned ct = 4u * cb + nt;
    if (ct < 54u) {
#pragma unroll
      for (int a = 0; a < 2; ++a) {
        u32x4 w;
#pragma unroll
        for (int e = 0; e < 4; ++e) w[e] = cvtpk(gelu_tanh(c.acc[nt][8 * a + 2 * e]), gelu_tanh(c.acc[nt][8 * a + 2 * e + 1]));
        *(u32x4*)(zF + ((size_t)((2u * ct + a) * 64u + rb) * 64u + lane) * 16u) = w;
      }
    }
  }
}

template <int NT> struct ExZ { static constexpr int N = 2 * NT; u32x4 r[N]; unsigned voff; rsrc_t srd; template <int J> DI void issue() { bload16(r[J], voff, srd, J * 65536u); } };
template <int NT> DI void spatial_unit(char* lds, unsigned lane, unsigned wid, unsigned rb, unsigned ct0, const unsigned char* wspF, const unsigned char* z2nT,
                                       const unsigned char* zF, const unsigned short* b_sp, unsigned char* uF) {
  GemmC<NT, 4> c; c.kid = 3;
  const unsigned ldsb = (unsigned)(size_t)lds;
#pragma unroll
  for (int q = 0; q < NT; ++q) { const unsigned sg = 2u * (wid * NT + q) + (lane >> 5), kg = sg / NT, nt = sg % NT;
    c.vw[q] = (kg * 27u + ct0 + nt) * 512u + (lane & 31u) * 16u; }
  c.wstride = 8u * 27u * 512u;
  c.m0w = rfl(ldsb + 1024u * NT * wid);
  c.vtr = ldsb + tr_lane<true, true>(lane, NT * 512u);
  c.srdW = mk_srd(z2nT, SEQ * FH * 2u);
  c.srdX = mk_srd(wspF, SEQ * SEQ * 2u);
  set_vx(c, rb * 1024u + lane * 16u);
  const float bias = bflo((unsigned)b_sp[rb * 32u + (lane & 31u)]);
  ExZ<NT> ex; ex.voff = (2u * ct0 * 64u + rb) * 1024u + lane * 16u; ex.srd = mk_srd(zF, SEQ * FF * 2u);
  asm volatile("s_waitcnt vmcnt(0)" :: "v"(bias) : "memory");
  gemm_run<NT, 4, true, 32, 8, 6>(c, ex);
#pragma unroll
  for (int j = 0; j < 2 * NT; ++j) asm volatile("" : "+v"(ex.r[j]));
#pragma unroll
  for (int nt = 0; nt < NT; ++nt)
#pragma unroll
    for (int a = 0; a < 2; ++a) {
      const unsigned ks = 2u * (ct0 + nt) + a;
      const u32x4 z1 = ex.r[2 * nt + a];
      u32x4 w;
#pragma unroll
      for (int e = 0; e < 4; ++e) w[e] = cvtpk(bflo(z1[e]) * (c.acc[nt][8 * a + 2 * e] + bias), bfhi(z1[e]) * (c.acc[nt][8 * a + 2 * e + 1] + bias));
      *(u32x4*)(uF + ((size_t)(ks * 64u + rb) * 64u + lane) * 16u) = w;
    }
}
__global__ __launch_bounds__(256, 1) void k_mid(const unsigned char* __restrict__ OF, const unsigned short* __restrict__ wo, unsigned char* __restrict__ sP,
                                                const unsigned char* __restrict__ wspF, const unsigned char* __restrict__ z2nT, const unsigned char* __restrict__ zF,
                                                const unsigned short* __restrict__ b_sp, unsigned char* __restrict__ uF) {
  extern __shared__ __attribute__((aligned(16))) char lds[];
  const unsigned lane = threadIdx.x & 63u, wid = rfl(threadIdx.x >> 6);
  const unsigned xcd = blockIdx.x & 7u, idx = blockIdx.x >> 3;
  if (idx >= 12u) {
    const unsigned i = idx - 12u, kh = xcd & 1u, rb128 = 4u * (xcd >> 1) + i / 5u, cb = i % 5u, rb = 4u * rb128 + wid;
    GemmC<4, 4> c; c.kid = 3;
    gemm_setup_rm<4, 4, false, true>(c, (unsigned)(size_t)lds, lane, wid, DM * 2u);
    const unsigned wofs = kh * 2048u * DM * 2u + cb * 256u;
    c.srdW = mk_srd((const char*)wo + wofs, NH * DH * DM * 2u - wofs);
    c.srdX = mk_srd(OF, SEQ * NH * DH * 2u);
    set_vx(c, (kh * 128u * 64u + rb) * 1024u + lane * 16u);
    NoEx ex; gemm_run<4, 4, false, 32, 8, 6>(c, ex);
#pragma unroll
    for (int nt = 0; nt < 4; ++nt)
#pragma unroll
      for (int g = 0; g < 4; ++g) {
        f32x4 w = {c.acc[nt][4 * g], c.acc[nt][4 * g + 1], c.acc[nt][4 * g + 2], c.acc[nt][4 * g + 3]};
        *(f32x4*)(sP + ((size_t)(((kh * 64u + rb) * 20u + 4u * cb + nt) * 4u + g) * 64u + lane) * 16u) = w;
      }
  } else {
    const unsigned rb128 = 4u * (xcd >> 1) + idx / 3u, j = 3u * (xcd & 1u) + idx % 3u, rb = 4u * rb128 + wid;
    if (j & 1u) spatial_unit<4>(lds, lane, wid, rb, 15u + 4u * (j >> 1), wspF, z2nT, zF, b_sp, uF);
    else        spatial_unit<5>(lds, lane, wid, rb, 5u * (j >> 1), wspF, z2nT, zF, b_sp, uF);
  }
}

struct ExO { static constexpr int N = 20; u32x4 r[N]; unsigned vs, vxx; rsrc_t srdS, srdXx;
  template <int J> DI void issue() { if constexpr (J < 16) bload16(r[J], vs, srdS, (J >> 3) * (64u * 20u * 4096u) + (J & 7) * 1024u); else bload16(r[J], vxx, srdXx, (J - 16) * 32u); } };
__global__ __launch_bounds__(256, 1) void k_out(const unsigned char* __restrict__ uF, const unsigned short* __restrict__ w_out, const unsigned char* __restrict__ sP,
                                                const unsigned short* __restrict__ x, unsigned short* __restrict__ out) {
  extern __shared__ __attribute__((aligned(16))) char lds[];
  const unsigned lane = threadIdx.x & 63u, wid = rfl(threadIdx.x >> 6);
  const unsigned xcd = blockIdx.x & 7u, idx = blockIdx.x >> 3;
  const unsigned rb128 = 4u * (xcd >> 1) + idx / 5u, cb = 5u * (xcd & 1u) + idx % 5u, rb = 4u * rb128 + wid;
  GemmC<2, 6> c; c.kid = 4;
  gemm_setup_rm<2, 6, false, true>(c, (unsigned)(size_t)lds, lane, wid, DM * 2u);
  c.srdW = mk_srd((const char*)w_out + cb * 128u, FH * DM * 2u - cb * 128u);
  c.srdX = mk_srd(uF, SEQ * FH * 2u);
  set_vx(c, rb * 1024u + lane * 16u);
  const unsigned row = rb * 32u + (lane & 31u), hi = lane >> 5;
  ExO ex; ex.vs = (rb * 20u + 2u * cb) * 4096u + lane * 16u; ex.vxx = row * 1280u + cb * 128u + hi * 16u;
  ex.srdS = mk_srd(sP, 2u * SEQ * DM * 4u); ex.srdXx = mk_srd(x, SEQ * DM * 2u);
  gemm_run<2, 6, false, 9, 9, 6>(c, ex);
#pragma unroll
  for (int j = 0; j < 20; ++j) asm volatile("" : "+v"(ex.r[j]));
#pragma unroll
  for (int nt = 0; nt < 2; ++nt) {
    const unsigned ct = 2u * cb + nt;
#pragma unroll
    for (int a = 0; a < 2; ++a) {
      const size_t eo = (size_t)row * DM + 32u * ct + 16u * a + 8u * hi;
      const u32x4 xv = ex.r[16 + 2 * nt + a];
      float sv[8];
#pragma unroll
      for (int g = 0; g < 2; ++g) {
        const f32x4 p0 = __builtin_bit_cast(f32x4, ex.r[4 * nt + 2 * a + g]), p1 = __builtin_bit_cast(f32x4, ex.r[8 + 4 * nt + 2 * a + g]);
#pragma unroll
        for (int e = 0; e < 4; ++e) sv[4 * g + e] = p0[e] + p1[e];
      }
      u32x4 w;
#pragma unroll
      for (int e = 0; e < 4; ++e) {
        const float b0 = c.acc[nt][8 * a + 2 * e], b1 = c.acc[nt][8 * a + 2 * e + 1];
        const float g0 = b0 * __builtin_amdgcn_rcpf(1.f + __builtin_amdgcn_exp2f(-1.4426950408889634f * b0));
        const float g1 = b1 * __builtin_amdgcn_rcpf(1.f + __builtin_amdgcn_exp2f(-1.4426950408889634f * b1));
        w[e] = cvtpk(bflo(xv[e]) + sv[2 * e] * g0, bfhi(xv[e]) + sv[2 * e + 1] * g1);
      }
      *(u32x4*)(out + eo) = w;
    }
  }
}

constexpr int NTILE = SEQ / 64;
constexpr unsigned KRS = 256u, KTILEB = 64u * KRS;
constexpr float THR = 8.0f;
#define KRING 0x0000u
#define VRING 0x8000u
#define WRING 0x0u
DI float w4_aone() { unsigned l; asm volatile("v_mbcnt_lo_u32_b32 %0, -1, 0\n\tv_mbcnt_hi_u32_b32 %0, -1, %0" : "=v"(l)); return l < 32u ? 1.f : 0.f; }
DI void negm_bcast(f32x16& negm, float aone, float neg_mhat) { asm volatile("s_nop 1\n\tv_mfma_f32_32x32x2_f32 %0, %1, %2, 0" : "+v"(negm) : "v"(aone), "v"(neg_mhat)); }
struct W4C {
  unsigned vkb[4], vvb, vksrc[2], vvsrc, m0k, m0v, sofk, sofv, ldsb;
  rsrc_t srdK, srdV;
  f32x16 S[2][2][2];
  f32x16 NEGM[2];
  bf16x8 Vf[4][4];
  bf16x8 Pk[2][4];
  bf16x4 vlo[4][4], vup[4][4];
  float mhat[2], lsum[2], fct[2]; bool pend;
  float rs0[2], rs1[2], pmx0[2], pmx1[2], rmax[2], delta[2];
  unsigned pm0[8], psf[8];
  u32x4 sw; unsigned vsl, vss, ssl, sss; rsrc_t srdSL, srdSS;
};
constexpr int AO_(int db, int qb) { return 16 * (2 * db + qb); }
constexpr int AQ_(int qb, int d0) { return 128 + 32 * qb + 4 * d0; }
constexpr int AK_(int kb, int d0) { return 192 + 32 * kb + 4 * d0; }
template <int I> DI void tr_read(W4C& c, unsigned vslot) {
  constexpr int ks = I >> 3, db = (I >> 1) & 3;
  if constexpr ((I & 1) == 0) asm volatile("ds_read_b64_tr_b16 %0, %1 offset:%2" : "=v"(c.vlo[db][ks]) : "v"(c.vvb + vslot), "i"(4096 * ks + 512 * db) : "memory");
  else                        asm volatile("ds_read_b64_tr_b16 %0, %1 offset:%2" : "=v"(c.vup[db][ks]) : "v"(c.vvb + vslot), "i"(4096 * ks + 2048 + 512 * db) : "memory");
}
template <int I> DI void k_read(W4C& c, unsigned slot) {
  constexpr int kb = I >> 3, d0 = I & 7;
  asm volatile("ds_read_b128 a[%c1:%c2], %0 offset:%c3" :: "v"(c.vkb[d0 & 3] + slot), "i"(AK_(kb, d0)), "i"(AK_(kb, d0) + 3), "i"(8192 * kb + 128 * (d0 >> 2)) : "memory", ACLOB_HI);
}
template <int X, bool FIRST, int M> DI void qk_mfma(W4C& c) {
  constexpr int d0 = M >> 2, kb = (M >> 1) & 1, qb = M & 1, ak = AK_(kb, d0), aq = AQ_(qb, d0);
  if constexpr (d0 == 0) {
    if constexpr (FIRST) asm volatile("v_mfma_f32_32x32x16_bf16 %0, a[%c1:%c2], a[%c3:%c4], 0" : "=&v"(c.S[X][kb][qb]) : "i"(ak), "i"(ak + 3), "i"(aq), "i"(aq + 3) : ACLOB);
    else                 asm volatile("v_mfma_f32_32x32x16_bf16 %0, a[%c2:%c3], a[%c4:%c5], %1" : "=&v"(c.S[X][kb][qb]) : "v"(c.NEGM[qb]), "i"(ak), "i"(ak + 3), "i"(aq), "i"(aq + 3) : ACLOB);
  } else asm volatile("v_mfma_f32_32x32x16_bf16 %0, a[%c1:%c2], a[%c3:%c4], %0" : "+v"(c.S[X][kb][qb]) : "i"(ak), "i"(ak + 3), "i"(aq), "i"(aq + 3) : ACLOB);
}
template <int M, bool ZERO> DI void pv_mfma(W4C& c) {
  constexpr int ks = M >> 3, db = (M >> 1) & 3, qb = M & 1, ao = AO_(db, qb);
  if constexpr (ZERO && ks == 0) asm volatile("v_mfma_f32_32x32x16_bf16 a[%c2:%c3], %0, %1, 0" :: "v"(c.Vf[db][ks]), "v"(c.Pk[qb][ks]), "i"(ao), "i"(ao + 15) : ACLOB_LO);
  else                         asm volatile("v_mfma_f32_32x32x16_bf16 a[%c2:%c3], %0, %1, a[%c2:%c3]" :: "v"(c.Vf[db][ks]), "v"(c.Pk[qb][ks]), "i"(ao), "i"(ao + 15) : ACLOB_LO);
}
template <int X, int QB, int KS, int P> DI void pack1(W4C& c) {
  u32x4 w = __builtin_bit_cast(u32x4, c.Pk[QB][KS]);
  w[P] = cvtpk(c.S[X][KS >> 1][QB][8 * (KS & 1) + 2 * P], c.S[X][KS >> 1][QB][8 * (KS & 1) + 2 * P + 1]);
  c.Pk[QB][KS] = __builtin_bit_cast(bf16x8, w);
}
template <int X, int J> DI void rowsum1(W4C& c) {
  constexpr int qb = J >> 4, ks = (J >> 2) & 3, p = J & 3;
  c.rs0[qb] = fadd_s(c.rs0[qb], c.S[X][ks >> 1][qb][8 * (ks & 1) + 2 * p]); c.rs1[qb] = fadd_s(c.rs1[qb], c.S[X][ks >> 1][qb][8 * (ks & 1) + 2 * p + 1]);
}
template <int X, bool FIRST, int M> DI void gapA(W4C& c, unsigned vslot, const int t) {
  constexpr int Xp = X ^ 1;
  qk_mfma<X, FIRST, M>(c);
  if constexpr (M >= 16 && M <= 31) {
    constexpr int q = (M - 16) >> 1;
    if constexpr (((M - 16) & 1) == 0) {
      if constexpr (q < 4) c.pm0[q] = c.m0k + (unsigned)((t + 2) & 1) * 0x4000u + 1024u * q; else c.pm0[q] = c.m0v + (unsigned)((t + 1) & 1) * 0x4000u + 1024u * (q - 4);
      asm volatile("" : "+s"(c.pm0[q]));
    } else {
      if constexpr (q < 4) c.psf[q] = KTILEB * (unsigned)(t + 2) + c.sofk + KRS * 4u * q; else c.psf[q] = KTILEB * (unsigned)(t + 1) + c.sofv + KRS * 8u * ((q - 4) >> 1) + 128u * ((q - 4) & 1);
      asm volatile("" : "+s"(c.psf[q]));
    }
  }
  if constexpr (M == 31) asm volatile("s_nop 4" : "+s"(c.pm0[0]), "+s"(c.pm0[1]), "+s"(c.pm0[2]), "+s"(c.pm0[3]), "+s"(c.pm0[4]), "+s"(c.pm0[5]), "+s"(c.pm0[6]), "+s"(c.pm0[7]),
                                                  "+s"(c.psf[0]), "+s"(c.psf[1]), "+s"(c.psf[2]), "+s"(c.psf[3]), "+s"(c.psf[4]), "+s"(c.psf[5]), "+s"(c.psf[6]), "+s"(c.psf[7]));
  if constexpr (!FIRST && M == 8) asm volatile("" :: "v"(c.NEGM[0]), "v"(c.NEGM[1]));
  if constexpr (!FIRST) {
    constexpr int base = M + (M >= 4) + (M >= 8) + (M >= 12) + (M >= 16) + (M >= 20) + (M >= 24);
    if constexpr (M <= 25) tr_read<base>(c, vslot);
    if constexpr (M == 3 || M == 7 || M == 11 || M == 15 || M == 19 || M == 23) tr_read<base + 1>(c, vslot);
    if constexpr (M < 16) {
      constexpr int e0 = 2 * M, e1 = 2 * M + 1;
      c.S[Xp][1][e0 >> 4][e0 & 15] = __builtin_amdgcn_exp2f(c.S[Xp][1][e0 >> 4][e0 & 15]);
      c.S[Xp][1][e1 >> 4][e1 & 15] = __builtin_amdgcn_exp2f(c.S[Xp][1][e1 >> 4][e1 & 15]);
      pack1<Xp, (M >> 3), ((M >> 2) & 1), (M & 3)>(c);
    } else {
      constexpr int cc = M - 16;
      pack1<Xp, (cc >> 3), 2 + ((cc >> 2) & 1), (cc & 3)>(c);
      rowsum1<Xp, 16 * (cc >> 3) + 8 + 4 * ((cc >> 2) & 1) + (cc & 3)>(c);
    }
  }
  GAP();
}
#define W4_VLO "+v"(c.vlo[0][0]), "+v"(c.vlo[0][1]), "+v"(c.vlo[0][2]), "+v"(c.vlo[0][3]), "+v"(c.vlo[1][0]), "+v"(c.vlo[1][1]), "+v"(c.vlo[1][2]), "+v"(c.vlo[1][3]), \
               "+v"(c.vlo[2][0]), "+v"(c.vlo[2][1]), "+v"(c.vlo[2][2]), "+v"(c.vlo[2][3]), "+v"(c.vlo[3][0]), "+v"(c.vlo[3][1]), "+v"(c.vlo[3][2]), "+v"(c.vlo[3][3])
#define W4_VUP "+v"(c.vup[0][0]), "+v"(c.vup[0][1]), "+v"(c.vup[0][2]), "+v"(c.vup[0][3]), "+v"(c.vup[1][0]), "+v"(c.vup[1][1]), "+v"(c.vup[1][2]), "+v"(c.vup[1][3]), \
               "+v"(c.vup[2][0]), "+v"(c.vup[2][1]), "+v"(c.vup[2][2]), "+v"(c.vup[2][3]), "+v"(c.vup[3][0]), "+v"(c.vup[3][1]), "+v"(c.vup[3][2]), "+v"(c.vup[3][3])
DI void v_concat(W4C& c) {
#pragma unroll
  for (int ks = 0; ks < 4; ++ks)
#pragma unroll
    for (int db = 0; db < 4; ++db) c.Vf[db][ks] = __builtin_shufflevector(c.vlo[db][ks], c.vup[db][ks], 0, 1, 2, 3, 4, 5, 6, 7);
}
DI void lsum_merge(W4C& c) {
#pragma unroll
  for (int qb = 0; qb < 2; ++qb) {
    const float rs = fadd_s(c.rs0[qb], c.rs1[qb]);
    auto pr = __builtin_amdgcn_permlane32_swap(__float_as_uint(rs), __float_as_uint(rs), false, false);
    c.lsum[qb] = fadd_s(c.lsum[qb], fadd_s(__uint_as_float(pr[0]), __uint_as_float(pr[1])));
  }
}
template <int X, bool FIRST, int VMA, int... Ms> DI void phaseA_(W4C& c, const int t, iseq<Ms...>) {
  const unsigned vslot = (unsigned)((t - 1) & 1) * 0x4000u;
  (gapA<X, FIRST, Ms>(c, vslot, t), ...);
  if constexpr (!FIRST) lsum_merge(c);
  if constexpr (FIRST) asm volatile("s_waitcnt vmcnt(0) lgkmcnt(0)\n\ts_barrier" ::: "memory");
  else { asm volatile("s_waitcnt vmcnt(%c16) lgkmcnt(0)" : W4_VLO : "i"(VMA) : "memory"); asm volatile("s_barrier" : W4_VUP :: "memory"); }
  GAP();
  if constexpr (!FIRST) v_concat(c);
}
template <int X, bool FIRST, int VMA = 0> DI void phaseA(W4C& c, const int t) { phaseA_<X, FIRST, VMA>(c, t, make_iseq<32>{}); }

template <int X, bool PEEL, bool ZERO1, bool DK, bool DV, bool KR, int SJ, int M> DI void gapB(W4C& c, const unsigned kslot_rd) {
  if constexpr (!PEEL) pv_mfma<M, ZERO1>(c);
  if constexpr (M < 4 && DK) dma16(c.pm0[M], c.vksrc[M & 1], c.srdK, c.psf[M]);
  else if constexpr (M >= 4 && M < 8 && DV) dma16(c.pm0[M], c.vvsrc, c.srdV, c.psf[M]);
  if constexpr ((SJ & 1) && M == 17) asm volatile("buffer_store_dwordx4 %0, %1, %2, %3 offen\n\ts_nop 1" : "+v"(c.sw) : "v"(c.vss), "s"(c.srdSS), "s"(c.sss) : "memory");
  if constexpr ((SJ & 2) && M == 18) asm volatile("buffer_load_dwordx4 %0, %1, %2, %3 offen" : "=v"(c.sw) : "v"(c.vsl), "s"(c.srdSL), "s"(c.ssl) : "memory");
  if constexpr (M >= 1 && M <= 16 && KR) k_read<M - 1>(c, kslot_rd);
  if constexpr (M >= 8 && M < 16) {
    constexpr int i = M - 8, q = i >> 2, r = 4 * (i & 3);
    if constexpr ((i & 3) == 0) { c.pmx0[q] = max3f(c.S[X][0][q][0], c.S[X][0][q][1], c.S[X][1][q][0]); c.pmx1[q] = max3f(c.S[X][0][q][2], c.S[X][0][q][3], c.S[X][1][q][1]);
                                  c.pmx0[q] = max3f(c.pmx0[q], c.S[X][1][q][2], c.S[X][1][q][3]); }
    else                        { c.pmx0[q] = max3f(c.pmx0[q], c.S[X][0][q][r], c.S[X][0][q][r + 1]); c.pmx1[q] = max3f(c.pmx1[q], c.S[X][0][q][r + 2], c.S[X][0][q][r + 3]);
                                  c.pmx0[q] = max3f(c.pmx0[q], c.S[X][1][q][r], c.S[X][1][q][r + 1]); c.pmx1[q] = max3f(c.pmx1[q], c.S[X][1][q][r + 2], c.S[X][1][q][r + 3]); }
  }
  if constexpr (M == 16 || M == 17) {
    constexpr int q = M - 16; const float mm = max2f(c.pmx0[q], c.pmx1[q]);
    auto pr = __builtin_amdgcn_permlane32_swap(__float_as_uint(mm), __float_as_uint(mm), false, false);
    c.rmax[q] = max2f(__uint_as_float(pr[0]), __uint_as_float(pr[1]));
  }
  if constexpr (M == 18) {
    if constexpr (PEEL) {
#pragma unroll
      for (int q = 0; q < 2; ++q) { c.mhat[q] = c.rmax[q]; c.lsum[q] = 0.f; c.fct[q] = 1.f;
#pragma unroll
        for (int kb = 0; kb < 2; ++kb) { _Pragma("unroll") for (int e = 0; e < 16; ++e) c.S[X][kb][q][e] = fsub_s(c.S[X][kb][q][e], c.rmax[q]); }
        negm_bcast(c.NEGM[q], w4_aone(), -c.mhat[q]);
      }
    } else {
      const bool grew = (c.rmax[0] > THR) | (c.rmax[1] > THR);
      if (__builtin_expect(__any((int)grew), 0)) {
#pragma unroll
        for (int q = 0; q < 2; ++q) { const float dl = max2f(c.rmax[q], 0.f); c.delta[q] = dl; c.mhat[q] = fadd_s(c.mhat[q], dl); c.fct[q] = __builtin_amdgcn_exp2f(-dl);
#pragma unroll
          for (int kb = 0; kb < 2; ++kb) { _Pragma("unroll") for (int e = 0; e < 16; ++e) c.S[X][kb][q][e] = fsub_s(c.S[X][kb][q][e], dl); }
          negm_bcast(c.NEGM[q], w4_aone(), -c.mhat[q]);
        }
        c.pend = true;
      }
    }
  }
  if constexpr (M == 18) { c.rs0[0] = c.rs0[1] = c.rs1[0] = c.rs1[1] = 0.f; }
  if constexpr (M >= 19) {
    constexpr int g = M - 19, e_lo = (32 * g) / 13, e_hi = (32 * (g + 1)) / 13;
    {
      constexpr int p_lo = g ? (32 * (g - 1)) / 13 : 0, p_hi = g ? (32 * g) / 13 : 0;
      if constexpr (p_lo + 0 < p_hi) { constexpr int e = p_lo + 0; if constexpr (e & 1) c.rs1[e >> 4] = fadd_s(c.rs1[e >> 4], c.S[X][0][e >> 4][e & 15]); else c.rs0[e >> 4] = fadd_s(c.rs0[e >> 4], c.S[X][0][e >> 4][e & 15]); }
      if constexpr (p_lo + 1 < p_hi) { constexpr int e = p_lo + 1; if constexpr (e & 1) c.rs1[e >> 4] = fadd_s(c.rs1[e >> 4], c.S[X][0][e >> 4][e & 15]); else c.rs0[e >> 4] = fadd_s(c.rs0[e >> 4], c.S[X][0][e >> 4][e & 15]); }
      if constexpr (p_lo + 2 < p_hi) { constexpr int e = p_lo + 2; if constexpr (e & 1) c.rs1[e >> 4] = fadd_s(c.rs1[e >> 4], c.S[X][0][e >> 4][e & 15]); else c.rs0[e >> 4] = fadd_s(c.rs0[e >> 4], c.S[X][0][e >> 4][e & 15]); }
    }
    if constexpr (e_lo + 0 < e_hi) c.S[X][0][(e_lo + 0) >> 4][(e_lo + 0) & 15] = __builtin_amdgcn_exp2f(c.S[X][0][(e_lo + 0) >> 4][(e_lo + 0) & 15]);
    if constexpr (e_lo + 1 < e_hi) c.S[X][0][(e_lo + 1) >> 4][(e_lo + 1) & 15] = __builtin_amdgcn_exp2f(c.S[X][0][(e_lo + 1) >> 4][(e_lo + 1) & 15]);
    if constexpr (e_lo + 2 < e_hi) c.S[X][0][(e_lo + 2) >> 4][(e_lo + 2) & 15] = __builtin_amdgcn_exp2f(c.S[X][0][(e_lo + 2) >> 4][(e_lo + 2) & 15]);
  }
  if constexpr (M == 31) asm volatile("" :: "v"(c.S[X][0][0]), "v"(c.S[X][0][1]), "v"(c.NEGM[0]), "v"(c.NEGM[1]), "v"(c.rs0[0]), "v"(c.rs0[1]), "v"(c.rs1[0]), "v"(c.rs1[1]));
  GAP();
}
template <int R> DI void o_scale1(float f0, float f1) {
  float t; asm volatile("v_accvgpr_read_b32 %0, a[%c2]\n\tv_mul_f32 %0, %1, %0\n\tv_accvgpr_write_b32 a[%c2], %0" : "=&v"(t) : "v"(((R >> 4) & 1) ? f1 : f0), "i"(R) : ACLOB_LO);
}
template <int... Rs> DI void o_scale(float f0, float f1, iseq<Rs...>) { (o_scale1<Rs>(f0, f1), ...); }
template <int X, bool PEEL, bool ZERO1, bool DK, bool DV, bool KR, int SJ, int... Ms> DI void phaseB_(W4C& c, const int t, iseq<Ms...>) {
  const unsigned kslot_rd = (unsigned)((t + 1) & 1) * 0x4000u;
  if constexpr (!PEEL) asm volatile("s_nop 1" ::: "memory");
  GAP();
  (gapB<X, PEEL, ZERO1, DK, DV, KR, SJ, Ms>(c, kslot_rd), ...);
#pragma unroll
  for (int e = (32 * 12) / 13; e < 32; ++e) { if (e & 1) c.rs1[e >> 4] = fadd_s(c.rs1[e >> 4], c.S[X][0][e >> 4][e & 15]); else c.rs0[e >> 4] = fadd_s(c.rs0[e >> 4], c.S[X][0][e >> 4][e & 15]); }
  if constexpr (!PEEL) {
    if (__builtin_expect(c.pend, 0)) {
      asm volatile("s_nop 7\n\ts_nop 7" ::: "memory");
      o_scale(c.fct[0], c.fct[1], make_iseq<128>{});
      asm volatile("s_nop 1" ::: "memory");
      c.lsum[0] = fmul_s(c.lsum[0], c.fct[0]); c.lsum[1] = fmul_s(c.lsum[1], c.fct[1]); c.pend = false;
    }
  }
  asm volatile("s_waitcnt lgkmcnt(0)" ::: "memory");
  GAP();
}
template <int X, bool PEEL, bool ZERO1, bool DK = true, bool DV = true, bool KR = true, int SJ = 0> DI void phaseB(W4C& c, const int t) { phaseB_<X, PEEL, ZERO1, DK, DV, KR, SJ>(c, t, make_iseq<32>{}); }
template <int... Is> DI void tr_reads(W4C& c, unsigned vslot, iseq<Is...>) { (tr_read<Is>(c, vslot), ...); }
template <int X, int... Cs> DI void packs(W4C& c, iseq<Cs...>) { (pack1<X, ((Cs >> 3) & 1), 2 * (Cs >> 4) + ((Cs >> 2) & 1), (Cs & 3)>(c), ...); }
template <int X, int J> DI void rowsum1_padded(W4C& c) { rowsum1<X, J>(c); asm volatile("s_nop 1"); }
template <int X, int... Js> DI void rowsums(W4C& c, iseq<Js...>) { (rowsum1_padded<X, Js>(c), ...); }
template <int... Ms> DI void pv_all(W4C& c, iseq<Ms...>) { (pv_mfma<Ms, false>(c), ...); }

struct QPC { unsigned vtr, vwsrc, m0w, sofw, vx; rsrc_t srdW, srdX; bf16x4 wl[8], wu[8]; u32x4 xv[4][8]; };
template <int F> DI void qp_tr(QPC& q, unsigned vaddr) {
  constexpr int ks = F >> 2, db = F & 3;
  asm volatile("ds_read_b64_tr_b16 %0, %1 offset:%2" : "=v"(q.wl[F & 7]) : "v"(vaddr), "i"(4096 * ks + 512 * db) : "memory");
  asm volatile("ds_read_b64_tr_b16 %0, %1 offset:%2" : "=v"(q.wu[F & 7]) : "v"(vaddr), "i"(4096 * ks + 256 + 512 * db) : "memory");
}
template <int T, int Q> DI void qp_g(QPC& q) {
  if constexpr (Q < 4) dma16(q.m0w + T * 0x4000u + 1024u * Q, q.vwsrc, q.srdW, q.sofw + T * 64u * 8192u + 8192u * 8u * (Q >> 1) + 128u * (Q & 1));
  else {
    constexpr int J = Q - 4, a = 128 + 32 * (T & 3) + 4 * J;
    if constexpr (T >= 4 && T < 8) asm volatile("buffer_load_dwordx4 %0, %1, %2, %3 offen offset:%c4" : "=v"(q.xv[T & 3][J]) : "v"(q.vx), "s"(q.srdX), "s"((unsigned)((4 * T + (J & 3)) * 65536)), "i"((J >> 2) * 1024) : "memory");
    else asm volatile("buffer_load_dwordx4 a[%c3:%c4], %0, %1, %2 offen offset:%c5" :: "v"(q.vx), "s"(q.srdX), "s"((unsigned)((4 * T + (J & 3)) * 65536)), "i"(a), "i"(a + 3), "i"((J >> 2) * 1024) : "memory", ACLOB_HI);
  }
}
template <int T, int... Q> DI void qp_grp(QPC& q, iseq<Q...>) { (qp_g<T, Q>(q), ...); }
template <int TT, int M, int LG> DI void qp_mfma(QPC& q) {
  constexpr int f = M >> 1, ks = f >> 2, db = f & 3, qb = M & 1, ao = AO_(db, qb), ax = 128 + 32 * (TT & 3) + 16 * qb + 4 * ks;
  if constexpr (qb == 0) asm volatile("s_waitcnt lgkmcnt(%c2)" : "+v"(q.wl[f & 7]), "+v"(q.wu[f & 7]) : "i"(LG) : "memory");
  const bf16x8 wf = __builtin_shufflevector(q.wl[f & 7], q.wu[f & 7], 0, 1, 2, 3, 4, 5, 6, 7);
  if constexpr (TT == 0 && ks == 0) asm volatile("v_mfma_f32_32x32x16_bf16 a[%c1:%c2], %0, a[%c3:%c4], 0" :: "v"(wf), "i"(ao), "i"(ao + 15), "i"(ax), "i"(ax + 3) : ACLOB);
  else if constexpr (TT >= 4 && TT < 8) asm volatile("v_mfma_f32_32x32x16_bf16 a[%c2:%c3], %0, %1, a[%c2:%c3]" :: "v"(wf), "v"(q.xv[TT & 3][4 * qb + ks]), "i"(ao), "i"(ao + 15) : ACLOB);
  else asm volatile("v_mfma_f32_32x32x16_bf16 a[%c1:%c2], %0, a[%c3:%c4], a[%c1:%c2]" :: "v"(wf), "i"(ao), "i"(ao + 15), "i"(ax), "i"(ax + 3) : ACLOB);
}
template <int T, int J> DI void qp_gap(QPC& q, W4C& c, unsigned vrd) {
  if constexpr (J < 8) { if constexpr (T >= 0) qp_mfma<T, 24 + J, (T == 9 ? 0 : 6)>(q); }
  else qp_mfma<T + 1, J - 8, 6>(q);
  if constexpr (T < 9 && (J & 1) == 0) qp_tr<(J >> 1)>(q, vrd);
  if constexpr (T <= 1 && J < 24) qp_g<2 * T + 4 + J / 12, J % 12>(q);
  if constexpr ((T == 2 || T == 3) && (J & 1) && J < 24) qp_g<T + 6, (J >> 1)>(q);
  if constexpr (T == 4 && (J & 1) && J < 8)            dma16(c.m0k + 1024u * (J >> 1), c.vksrc[(J >> 1) & 1], c.srdK, c.sofk + KRS * 4u * (J >> 1));
  if constexpr (T == 4 && (J & 1) && J >= 8 && J < 16)  dma16(c.m0v + 1024u * ((J - 8) >> 1), c.vvsrc, c.srdV, c.sofv + KRS * 8u * ((J - 8) >> 2) + 128u * ((J >> 1) & 1));
  if constexpr (T == 4 && (J & 1) && J >= 16 && J < 24) dma16(c.m0k + 0x4000u + 1024u * ((J - 16) >> 1), c.vksrc[(J >> 1) & 1], c.srdK, KTILEB + c.sofk + KRS * 4u * ((J - 16) >> 1));
  if constexpr (T == 9 && J >= 6) { k_read<8 * (J - 6)>(c, 0u); k_read<8 * (J - 6) + 1>(c, 0u); k_read<8 * (J - 6) + 2>(c, 0u); k_read<8 * (J - 6) + 3>(c, 0u);
                                    k_read<8 * (J - 6) + 4>(c, 0u); k_read<8 * (J - 6) + 5>(c, 0u); k_read<8 * (J - 6) + 6>(c, 0u); k_read<8 * (J - 6) + 7>(c, 0u); }
  GAP();
}
template <int T, int VM, int... Js> DI void qp_step_(QPC& q, W4C& c, iseq<Js...>) {
  const unsigned vrd = q.vtr + (T + 1) * 0x4000u;
  constexpr int u = (T + 1) & 3;
  if constexpr (T + 1 >= 4 && T + 1 < 8) asm volatile("s_waitcnt vmcnt(%c8)\n\ts_barrier" : "+v"(q.xv[u][0]), "+v"(q.xv[u][1]), "+v"(q.xv[u][2]), "+v"(q.xv[u][3]), "+v"(q.xv[u][4]), "+v"(q.xv[u][5]), "+v"(q.xv[u][6]), "+v"(q.xv[u][7]) : "i"(VM) : "memory");
  else if constexpr (T < 9) asm volatile("s_waitcnt vmcnt(%c0)\n\ts_barrier" :: "i"(VM) : "memory");
  GAP();
  (qp_gap<T, Js>(q, c, vrd), ...);
}
template <int T, int VM> DI void qp_step(QPC& q, W4C& c) { qp_step_<T, VM>(q, c, make_iseq<(T == 9 ? 8 : 32)>{}); }
template <int R> DI u32x4 acc_pack8(float f) {
  float o[8];
  asm volatile("v_accvgpr_read_b32 %0, a[%c8]\n\tv_accvgpr_read_b32 %1, a[%c9]\n\tv_accvgpr_read_b32 %2, a[%c10]\n\tv_accvgpr_read_b32 %3, a[%c11]\n\t"
               "v_accvgpr_read_b32 %4, a[%c12]\n\tv_accvgpr_read_b32 %5, a[%c13]\n\tv_accvgpr_read_b32 %6, a[%c14]\n\tv_accvgpr_read_b32 %7, a[%c15]"
               : "=v"(o[0]), "=v"(o[1]), "=v"(o[2]), "=v"(o[3]), "=v"(o[4]), "=v"(o[5]), "=v"(o[6]), "=v"(o[7])
               : "i"(R), "i"(R + 1), "i"(R + 2), "i"(R + 3), "i"(R + 4), "i"(R + 5), "i"(R + 6), "i"(R + 7));
  u32x4 w;
#pragma unroll
  for (int e = 0; e < 4; ++e) w[e] = cvtpk(o[2 * e] * f, o[2 * e + 1] * f);
  return w;
}
template <int I> DI void q_conv1(float C) {
  constexpr int qb = I >> 3, d0 = I & 7, aq = AQ_(qb, d0);
  const u32x4 w = acc_pack8<AO_(d0 >> 1, qb) + 8 * (d0 & 1)>(C);
  asm volatile("v_accvgpr_write_b32 a[%c4], %0\n\tv_accvgpr_write_b32 a[%c5], %1\n\tv_accvgpr_write_b32 a[%c6], %2\n\tv_accvgpr_write_b32 a[%c7], %3"
               :: "v"(w[0]), "v"(w[1]), "v"(w[2]), "v"(w[3]), "i"(aq), "i"(aq + 1), "i"(aq + 2), "i"(aq + 3) : ACLOB_HI);
}
template <int... Is> DI void q_conv(float C, iseq<Is...>) { (q_conv1<Is>(C), ...); }
template <int I> DI void o_store1(unsigned char* dst, float il0, float il1) {
  constexpr int qb = I >> 3, j = I & 7;
  const u32x4 w = acc_pack8<AO_(j >> 1, qb) + 8 * (j & 1)>(qb ? il1 : il0);
  *(u32x4*)(dst + qb * 1024u + (size_t)j * 65536u) = w;
}
template <int... Is> DI void o_store(unsigned char* dst, float il0, float il1, iseq<Is...>) { (o_store1<Is>(dst, il0, il1), ...); }
DI void gload16(u32x4& r, const void* p) { asm volatile("global_load_dwordx4 %0, %1, off" : "=v"(r) : "v"(p) : "memory"); }
DI void gstore16(void* p, const u32x4& r) { asm volatile("global_store_dwordx4 %0, %1, off\n\ts_nop 1" :: "v"(p), "v"(r) : "memory"); }

__global__ __launch_bounds__(256, 1)
void k_attn(const unsigned char* __restrict__ xnF, const unsigned short* __restrict__ wq, const unsigned short* __restrict__ Kc, const unsigned short* __restrict__ Vc,
            unsigned char* __restrict__ OF, const unsigned char* __restrict__ zF, unsigned char* __restrict__ z2nT,
            const unsigned short* __restrict__ w_sp, unsigned char* __restrict__ wspF,
            const unsigned short* __restrict__ wo, const unsigned short* __restrict__ w_out) {
  extern __shared__ __attribute__((aligned(16))) char lds[];
  const unsigned tid = threadIdx.x, lane = tid & 63u, wid = rfl(tid >> 6), l32 = lane & 31u, hi = lane >> 5;
  constexpr int T = NTILE;
  const float C = 0.12751743082f;
  const unsigned xcd = blockIdx.x & 7u, idx = blockIdx.x >> 3;
  const unsigned h = 8u * (xcd >> 1) + (idx & 7u), y = 4u * (xcd & 1u) + (idx >> 3);
  const unsigned rb0 = 8u * y + 2u * wid;
  W4C c; QPC q;
  TS(2, 0);
  c.ldsb = (unsigned)(size_t)lds;
#pragma unroll
  for (int j = 0; j < 4; ++j) c.vkb[j] = c.ldsb + KRING + 256u * l32 + 16u * ((2u * j + hi) ^ (l32 & 7u));
  c.vvb = c.ldsb + VRING + tr_lane<false, false>(lane, 0u);
#pragma unroll
  for (int par = 0; par < 2; ++par) c.vksrc[par] = KRS * (lane >> 4) + 16u * ((lane & 15u) ^ (4u * par + (lane >> 4)));
  c.vvsrc = KRS * ((lane >> 2) & 7u) + 64u * hi + 16u * (lane & 3u);
  c.m0k = rfl(c.ldsb + KRING + 4096u * wid); c.m0v = rfl(c.ldsb + VRING + 4096u * wid);
  c.sofk = KRS * 16u * wid; c.sofv = KRS * 16u * wid;
  c.srdK = mk_srd((const char*)Kc + (size_t)h * SEQ * 256u, SEQ * 256u);
  c.srdV = mk_srd((const char*)Vc + (size_t)h * SEQ * 256u, SEQ * 256u);
  q.vtr = c.ldsb + WRING + tr_lane<true, true>(lane, 2048u);
  q.vwsrc = 8192u * ((lane >> 2) & 7u) + 64u * hi + 16u * (lane & 3u);
  q.m0w = rfl(c.ldsb + WRING + 4096u * wid); q.sofw = 8192u * 16u * wid;
  q.vx = rb0 * 1024u + lane * 16u;
  q.srdW = mk_srd((const char*)wq + h * 256u, DM * 8192u - h * 256u);
  q.srdX = mk_srd(xnF, SEQ * DM * 2u);
  const unsigned srow = blockIdx.x * 8u + 2u * wid + hi;
  u32x4 zv[4];
#pragma unroll
  for (int i = 0; i < 4; ++i) { const unsigned pc = l32 + 32u * i, p = pc < 108u ? pc : 107u;
    gload16(zv[i], zF + ((size_t)((54u + (p >> 1)) * 64u + (srow >> 5)) * 64u + (p & 1u) * 32u + (srow & 31u)) * 16u); }
  asm volatile("s_nop 4" ::: "memory");
  qp_grp<0>(q, make_iseq<12>{}); qp_grp<1>(q, make_iseq<12>{});
  asm volatile("s_waitcnt vmcnt(24)" : "+v"(zv[0]), "+v"(zv[1]), "+v"(zv[2]), "+v"(zv[3]) :: "memory");
  {
    const bool v3 = l32 < 12u;
    float f[32], s = 0.f;
#pragma unroll
    for (int i = 0; i < 4; ++i)
#pragma unroll
      for (int e = 0; e < 4; ++e) { f[8 * i + 2 * e] = bflo(zv[i][e]); f[8 * i + 2 * e + 1] = bfhi(zv[i][e]); const float t2 = f[8 * i + 2 * e] + f[8 * i + 2 * e + 1]; s += (i < 3 || v3) ? t2 : 0.f; }
#pragma unroll
    for (int o = 16; o; o >>= 1) s += __shfl_xor(s, o);
    const float mean = s * (1.f / FH); float qq = 0.f;
#pragma unroll
    for (int i = 0; i < 32; ++i) { f[i] -= mean; qq += (i < 24 || v3) ? f[i] * f[i] : 0.f; }
#pragma unroll
    for (int o = 16; o; o >>= 1) qq += __shfl_xor(qq, o);
    const float rstd = 1.f / sqrtf(qq * (1.f / FH) + 1e-5f);
#pragma unroll
    for (int i = 0; i < 4; ++i) { const unsigned pc = l32 + 32u * i, p = pc < 108u ? pc : 107u;
      u32x4 w;
#pragma unroll
      for (int e = 0; e < 4; ++e) w[e] = cvtpk(f[8 * i + 2 * e] * rstd, f[8 * i + 2 * e + 1] * rstd);
      gstore16(z2nT + ((size_t)(blockIdx.x * 27u + (p >> 2)) * 8u + (srow & 7u)) * 64u + (p & 3u) * 16u, w); }
  }
  TS(2, 4); TS(2, 5);
  asm volatile("s_waitcnt lgkmcnt(0)" ::: "memory");
  qp_step<-1, 16>(q, c); qp_step<0, 28>(q, c); qp_step<1, 36>(q, c); qp_step<2, 48>(q, c); qp_step<3, 48>(q, c); qp_step<4, 48>(q, c);
  qp_step<5, 48>(q, c); qp_step<6, 36>(q, c); qp_step<7, 24>(q, c); qp_step<8, 8>(q, c); qp_step<9, 0>(q, c);
  asm volatile("s_nop 7\n\ts_nop 7" ::: "memory");
  q_conv(C, make_iseq<16>{});
  asm volatile("s_waitcnt lgkmcnt(0)\n\ts_nop 1" ::: "memory");
  GAP();
  TS(2, 1);
  c.mhat[0] = c.mhat[1] = 0.f; c.lsum[0] = c.lsum[1] = 0.f; c.fct[0] = c.fct[1] = 1.f; c.pend = false;
  { const unsigned srb = blockIdx.x >> 2, ks0 = (blockIdx.x & 3u) * 32u + wid * 8u;
    c.vsl = (srb * 32u + l32) * 4096u + ks0 * 32u + hi * 16u; c.vss = (ks0 * 64u + srb) * 1024u + lane * 16u;
    c.srdSL = mk_srd(w_sp, SEQ * SEQ * 2u); c.srdSS = mk_srd(wspF, SEQ * SEQ * 2u); c.ssl = 0u; c.sss = 0u; }
  phaseA<0, true>(c, 0);  phaseB<0, true, false, true, true, true, 2>(c, 0);
  phaseA<1, false, 1>(c, 1); phaseB<1, false, true>(c, 1);
  { const unsigned gw = blockIdx.x * 4u + wid;
    const bool isw = gw < 640u; const unsigned go = isw ? gw : (gw < 775u ? gw - 640u : 134u);
    const rsrc_t srdP = mk_srd(isw ? wo : w_out, isw ? NH * DH * DM * 2u : FH * DM * 2u);
    asm volatile("s_mov_b32 m0, %0\n\ts_nop 4\n\tbuffer_load_dword %1, %2, %3 offen lds\n\tbuffer_load_dword %1, %2, %3 offen offset:64 lds"
                 :: "s"(c.ldsb + 0x10000u + wid * 1024u), "v"(lane * 128u), "s"(srdP), "s"(go * 8192u) : "m0", "memory"); }
  for (int t = 2; t < T - 2; t += 2) {
    const unsigned fi = (unsigned)(t >> 1), fl = fi < 7u ? fi : 7u, fs = fi - 1u < 7u ? fi - 1u : 7u;
    c.ssl = fl * 32u; c.sss = fs * 65536u;
    phaseA<0, false>(c, t);        phaseB<0, false, false, true, true, true, 3>(c, t);
    phaseA<1, false, 2>(c, t + 1); phaseB<1, false, false>(c, t + 1);
  }
  TS(2, 2);
  phaseA<0, false>(c, T - 2); phaseB<0, false, false, false, true, true, 1>(c, T - 2);
  phaseA<1, false>(c, T - 1); phaseB<1, false, false, false, false, false>(c, T - 1);
  {
    const unsigned vslot = (unsigned)((T - 1) & 1) * 0x4000u;
    tr_reads(c, vslot, make_iseq<32>{});
#pragma unroll
    for (int e = 0; e < 32; ++e) c.S[1][1][e >> 4][e & 15] = __builtin_amdgcn_exp2f(c.S[1][1][e >> 4][e & 15]);
    packs<1>(c, make_iseq<32>{});
    rowsums<1>(c, iseq<8, 9, 10, 11, 12, 13, 14, 15, 24, 25, 26, 27, 28, 29, 30, 31>{});
    lsum_merge(c);
    asm volatile("s_waitcnt lgkmcnt(0)" : W4_VLO :: "memory"); asm volatile("" : W4_VUP :: "memory");
    GAP();
    v_concat(c);
    asm volatile("s_nop 1" ::: "memory");
    pv_all(c, make_iseq<32>{});
    asm volatile("s_nop 7\n\ts_nop 7" ::: "memory");
  }
  {
    unsigned lb; asm volatile("v_mbcnt_lo_u32_b32 %0, -1, 0\n\tv_mbcnt_hi_u32_b32 %0, -1, %0" : "=v"(lb));
    const float il0 = __builtin_amdgcn_rcpf(c.lsum[0]), il1 = __builtin_amdgcn_rcpf(c.lsum[1]);
    o_store(OF + ((size_t)(8u * h * 64u + rb0) * 64u + lb) * 16u, il0, il1, make_iseq<16>{});
  }
  TS(2, 3);
}

extern "C" void kernel_launch(void* const* d_in, const int* in_sizes, int n_in, void* d_out, int out_size, void* d_ws, size_t ws_size, hipStream_t stream) {
  const unsigned short* x = (const unsigned short*)d_in[0];
  const unsigned short* Kc = (const unsigned short*)d_in[1];
  const unsigned short* Vc = (const unsigned short*)d_in[2];
  const unsigned short* wq = (const unsigned short*)d_in[3];
  const unsigned short* wo = (const unsigned short*)d_in[4];
  const unsigned short* w_in = (const unsigned short*)d_in[6];
  const unsigned short* w_sp = (const unsigned short*)d_in[7];
  const unsigned short* b_sp = (const unsigned short*)d_in[8];
  const unsigned short* w_out = (const unsigned short*)d_in[9];
  unsigned char* ws = (unsigned char*)d_ws;
  (void)hipFuncSetAttribute((const void*)k_attn, hipFuncAttributeMaxDynamicSharedMemorySize, 163840);
  (void)hipFuncSetAttribute((const void*)k_zgemm, hipFuncAttributeMaxDynamicSharedMemorySize, 163840);
  (void)hipFuncSetAttribute((const void*)k_mid, hipFuncAttributeMaxDynamicSharedMemorySize, 163840);
  (void)hipFuncSetAttribute((const void*)k_out, hipFuncAttributeMaxDynamicSharedMemorySize, 110592);
  hipLaunchKernelGGL(k_ln, dim3(256), dim3(256), 0, stream, x, ws + WS_XN, w_in, wq);
  hipLaunchKernelGGL(k_zgemm, dim3(224), dim3(256), 163840, stream, ws + WS_XN, w_in, ws + WS_Z);
  hipLaunchKernelGGL(k_attn, dim3(256), dim3(256), 163840, stream, ws + WS_XN, wq, Kc, Vc, ws + WS_O, ws + WS_Z, ws + WS_Z2N, w_sp, ws + WS_WSP, wo, w_out);
  hipLaunchKernelGGL(k_mid, dim3(256), dim3(256), 163840, stream, ws + WS_O, wo, ws + WS_SP, ws + WS_WSP, ws + WS_Z2N, ws + WS_Z, b_sp, ws + WS_U);
  hipLaunchKernelGGL(k_out, dim3(160), dim3(256), 110592, stream, ws + WS_U, w_out, ws + WS_SP, x, (unsigned short*)d_out);
}
```

```cpp
#include <hip/hip_runtime.h>
#include <hip/hip_bf16.h>
#include <utility>

typedef float    f32x16 __attribute__((ext_vector_type(16)));
typedef float    f32x2  __attribute__((ext_vector_type(2)));
typedef float    f32x4  __attribute__((ext_vector_type(4)));
typedef __bf16   bf16x8 __attribute__((ext_vector_type(8)));
typedef __bf16   bf16x4 __attribute__((ext_vector_type(4)));
typedef __bf16   bf16x2 __attribute__((ext_vector_type(2)));
typedef unsigned u32x4  __attribute__((ext_vector_type(4)));
typedef unsigned u32x2  __attribute__((ext_vector_type(2)));
template <int... Is> using iseq = std::integer_sequence<int, Is...>;
template <int N> using make_iseq = std::make_integer_sequence<int, N>;
typedef __amdgpu_buffer_rsrc_t rsrc_t;

#define DI __device__ __forceinline__
#define GAP() __builtin_amdgcn_sched_barrier(0)
#define SRD_WORD3 0x00020000
#define A10(x) "a" #x "0", "a" #x "1", "a" #x "2", "a" #x "3", "a" #x "4", "a" #x "5", "a" #x "6", "a" #x "7", "a" #x "8", "a" #x "9"
#define ACLOB_LO "a0", "a1", "a2", "a3", "a4", "a5", "a6", "a7", "a8", "a9", A10(1), A10(2), A10(3), A10(4), A10(5), A10(6), A10(7), A10(8), A10(9), A10(10), A10(11), \
                 "a120", "a121", "a122", "a123", "a124", "a125", "a126", "a127"
#define ACLOB_HI "a128", "a129", A10(13), A10(14), A10(15), A10(16), A10(17), A10(18), A10(19), A10(20), A10(21), A10(22), A10(23), A10(24), \
                 "a250", "a251", "a252", "a253", "a254", "a255"
#define ACLOB ACLOB_LO, ACLOB_HI

constexpr int SEQ = 2048, DM = 640, NH = 32, DH = 128, FF = 1728, FH = 864;
constexpr size_t WS_XN  = 0;
constexpr size_t WS_Z   = WS_XN  + (size_t)SEQ * DM * 2;
constexpr size_t WS_O   = WS_Z   + (size_t)SEQ * FF * 2;
constexpr size_t WS_Z2N = WS_O   + (size_t)SEQ * NH * DH * 2;
constexpr size_t WS_WSP = WS_Z2N + (size_t)SEQ * FH * 2;
constexpr size_t WS_SP  = WS_WSP + (size_t)SEQ * SEQ * 2;
constexpr size_t WS_U   = WS_SP  + (size_t)2 * SEQ * DM * 4;
constexpr size_t WS_END = WS_U   + (size_t)SEQ * FH * 2;

DI unsigned rfl(unsigned x) { return __builtin_amdgcn_readfirstlane(x); }
DI unsigned cvtpk(float lo, float hi) { f32x2 v = {lo, hi}; bf16x2 b = __builtin_convertvector(v, bf16x2); return __builtin_bit_cast(unsigned, b); }
DI float bflo(unsigned w) { return __uint_as_float(w << 16); }
DI float bfhi(unsigned w) { return __uint_as_float(w & 0xffff0000u); }
DI rsrc_t mk_srd(const void* p, unsigned bytes) { return __builtin_amdgcn_make_buffer_rsrc((void*)p, (short)0, (int)bytes, SRD_WORD3); }
DI float max3f(float a, float b, float c) { float r; asm("v_max3_f32 %0, %1, %2, %3" : "=v"(r) : "v"(a), "v"(b), "v"(c)); return r; }
DI float max2f(float a, float b) { float r; asm("v_max_f32_e32 %0, %1, %2" : "=v"(r) : "v"(a), "v"(b)); return r; }
DI float fadd_s(float a, float b) { float r; asm("v_add_f32_e32 %0, %1, %2" : "=v"(r) : "v"(a), "v"(b)); return r; }
DI float fmul_s(float a, float b) { float r; asm("v_mul_f32_e32 %0, %1, %2" : "=v"(r) : "v"(a), "v"(b)); return r; }
DI float fsub_s(float a, float b) { float r; asm("v_sub_f32_e32 %0, %1, %2" : "=v"(r) : "v"(a), "v"(b)); return r; }
template <int N> DI void wait_vm_bar() { asm volatile("s_waitcnt vmcnt(%c0) lgkmcnt(0)\n\ts_barrier" :: "i"(N > 63 ? 63 : N) : "memory"); }
DI void dma16(unsigned m0v, unsigned voff, rsrc_t srd, unsigned soff) {
  asm volatile("s_mov_b32 m0, %0\n\ts_nop 0\n\tbuffer_load_dwordx4 %1, %2, %3 offen lds" :: "s"(m0v), "v"(voff), "s"(srd), "s"(soff) : "m0", "memory");
}
DI void touch8k(const char* p) { unsigned d0, d1; asm volatile("global_load_dword %0, %2, off\n\tglobal_load_dword %1, %2, off offset:64" : "=&v"(d0), "=&v"(d1) : "v"(p) : "memory"); }
template <bool NATK, bool SWAPN> DI unsigned tr_lane(unsigned lane, unsigned halfstride) {
  const unsigned c = lane & 3u, cs = SWAPN ? (((c & 1u) << 1) | (c >> 1)) : c, hi = lane >> 5;
  return (cs << 3) | (((lane >> 2) & 3u) << 6) | (((lane >> 4) & 1u) << 5) | (NATK ? hi * halfstride : (hi << 8));
}

#define TS(k, i)
__global__ __launch_bounds__(256) void k_ln(const unsigned short* __restrict__ x, unsigned char* __restrict__ xnF, const unsigned short* __restrict__ w_in, const unsigned short* __restrict__ wq) {
  const unsigned lane = threadIdx.x & 63u, wid = threadIdx.x >> 6, l = lane & 31u;
  const unsigned row = blockIdx.x * 8u + wid * 2u + (lane >> 5);
  const u32x4* src = (const u32x4*)(x + (size_t)row * DM);
  u32x4 v[3]; v[0] = src[l]; v[1] = src[l + 32]; v[2] = (l < 16u) ? src[l + 64] : (u32x4){0u, 0u, 0u, 0u};
  float f[24]; float s = 0.f;
#pragma unroll
  for (int i = 0; i < 3; ++i)
#pragma unroll
    for (int e = 0; e < 4; ++e) { f[8 * i + 2 * e] = bflo(v[i][e]); f[8 * i + 2 * e + 1] = bfhi(v[i][e]); s += f[8 * i + 2 * e] + f[8 * i + 2 * e + 1]; }
#pragma unroll
  for (int o = 16; o; o >>= 1) s += __shfl_xor(s, o);
  const float mean = s * (1.f / DM); float q = 0.f;
#pragma unroll
  for (int i = 0; i < 24; ++i) { f[i] -= mean; if (i < 16 || l < 16u) q += f[i] * f[i]; }
#pragma unroll
  for (int o = 16; o; o >>= 1) q += __shfl_xor(q, o);
  const float rstd = 1.f / sqrtf(q * (1.f / DM) + 1e-5f);
#pragma unroll
  for (int i = 0; i < 3; ++i) {
    const unsigned c = l + 32u * i;
    if (i < 2 || l < 16u) {
      u32x4 w;
#pragma unroll
      for (int e = 0; e < 4; ++e) w[e] = cvtpk(f[8 * i + 2 * e] * rstd, f[8 * i + 2 * e + 1] * rstd);
      *(u32x4*)(xnF + ((size_t)((c >> 1) * 64u + (row >> 5)) * 64u + (c & 1u) * 32u + (row & 31u)) * 16u) = w;
    }
  }
  { const unsigned gw = blockIdx.x * 4u + wid;
    if (gw < 270u) touch8k((const char*)w_in + gw * 8192u + lane * 128u); else if (gw < 910u) touch8k((const char*)wq + (gw - 270u) * 8192u + lane * 128u); }
}

constexpr int LA = 6;
template <int NT, int KS> struct GemmC {
  static constexpr int P = NT * KS / 4, TILEB = NT * KS * 1024, G = P + KS, NM = NT * KS;
  unsigned vw[P];
  unsigned vx[KS];
  unsigned vtr;
  unsigned m0w;
  unsigned wstride;
  rsrc_t srdW, srdX;
  int kid;
  f32x16 acc[NT];
};
struct NoEx { static constexpr int N = 0; template <int J> DI void issue() {} };
template <int KS> constexpr int GX(int u, int ks) { return 32 + (u * KS + ks) * 4; }
template <int NT, int KS, bool NATK, int AS, int F> DI void g_tr(unsigned vaddr) {
  constexpr int ks = F / NT, nt = F % NT, a = 4 * AS;
  constexpr int o0 = NATK ? ((2 * ks) * NT + nt) * 512 : ((2 * ks) * NT + nt) * 512, o1 = NATK ? o0 + 256 : o0 + NT * 512;
  asm volatile("ds_read_b64_tr_b16 a[%c1:%c2], %0 offset:%c5\n\tds_read_b64_tr_b16 a[%c3:%c4], %0 offset:%c6" :: "v"(vaddr), "i"(a), "i"(a + 1), "i"(a + 2), "i"(a + 3), "i"(o0), "i"(o1) : "memory", ACLOB);
}
template <int NT, int KS, int XI, int KSI> DI void g_xload(GemmC<NT, KS>& c, unsigned soff) {
  constexpr int a = GX<KS>(XI, KSI);
  asm volatile("buffer_load_dwordx4 a[%c3:%c4], %0, %1, %2 offen" :: "v"(c.vx[KSI]), "s"(c.srdX), "s"(soff), "i"(a), "i"(a + 3) : "memory", ACLOB);
}
template <int NT, int KS, int AS, int XI, bool ZERO, int LG, int M> DI void g_mfma(GemmC<NT, KS>& c) {
  constexpr int ks = M / NT, nt = M % NT, a = 4 * AS, b = GX<KS>(XI, ks);
  if constexpr (ZERO && ks == 0) asm volatile("s_waitcnt lgkmcnt(%c5)\n\tv_mfma_f32_32x32x16_bf16 %0, a[%c1:%c2], a[%c3:%c4], 0" : "=v"(c.acc[nt]) : "i"(a), "i"(a + 3), "i"(b), "i"(b + 3), "i"(LG) : "memory", ACLOB);
  else asm volatile("s_waitcnt lgkmcnt(%c5)\n\tv_mfma_f32_32x32x16_bf16 %0, a[%c1:%c2], a[%c3:%c4], %0" : "+v"(c.acc[nt]) : "i"(a), "i"(a + 3), "i"(b), "i"(b + 3), "i"(LG) : "memory", ACLOB);
}
template <int NT, int KS, int D, int GS, int Q> DI void g_vmem(GemmC<NT, KS>& c, unsigned g) {
  constexpr int P = GemmC<NT, KS>::P, G = GemmC<NT, KS>::G, gi = Q / G, q = Q % G, slot = (GS + gi) % D;
  if constexpr (q < P) dma16(c.m0w + slot * GemmC<NT, KS>::TILEB + 1024u * q, c.vw[q], c.srdW, (g + gi) * c.wstride);
  else g_xload<NT, KS, slot, q - P>(c, (g + gi) * (KS * 65536u));
}
template <int NT, int KS, int D, int... Q> DI void g_pre(GemmC<NT, KS>& c, iseq<Q...>) { (g_vmem<NT, KS, D, 0, Q>(c, 0u), ...); }
template <int NT, int KS, bool NATK, int D, int TM, bool FIRST, bool LAST, int GS, int NG, bool EXS, class EX, int J>
DI void g_gap(GemmC<NT, KS>& c, EX& ex, unsigned vrd, unsigned g) {
  constexpr int NM = NT * KS, NOP = NG * GemmC<NT, KS>::G, OPG = (NOP + NM - 1) / NM, T1 = (TM + 1) % D;
  if constexpr (J < LA) { if constexpr (!FIRST) g_mfma<NT, KS, (TM * NM + NM - LA + J) & 7, TM, false, (LAST ? 2 * (LA - 1 - J) : 2 * (LA - 1)), NM - LA + J>(c); }
  else if constexpr (!LAST) g_mfma<NT, KS, (T1 * NM + J - LA) & 7, T1, FIRST, 2 * (LA - 1), J - LA>(c);
  if constexpr (!LAST) g_tr<NT, KS, NATK, (T1 * NM + J) & 7, J>(vrd);
  if constexpr (OPG * J < NOP) g_vmem<NT, KS, D, GS, OPG * J>(c, g);
  if constexpr (OPG == 2 && 2 * J + 1 < NOP) g_vmem<NT, KS, D, GS, 2 * J + 1>(c, g);
  if constexpr (EXS) { if constexpr (2 * J < EX::N) ex.template issue<2 * J>(); if constexpr (2 * J + 1 < EX::N) ex.template issue<2 * J + 1>(); }
  GAP();
}
template <int NT, int KS, bool NATK, int D, int TM, bool FIRST, bool LAST, int GS, int NG, bool EXS, int VM, class EX, int... Js>
DI void g_step_(GemmC<NT, KS>& c, EX& ex, unsigned g, iseq<Js...>) {
  const unsigned vrd = c.vtr + ((TM + 1) % D) * GemmC<NT, KS>::TILEB;
  if constexpr (!LAST) asm volatile("s_waitcnt vmcnt(%c0)\n\ts_barrier" :: "i"(VM > 63 ? 63 : VM) : "memory");
  if constexpr (FIRST) TS(c.kid, 1);
  GAP();
  (g_gap<NT, KS, NATK, D, TM, FIRST, LAST, GS, NG, EXS, EX, Js>(c, ex, vrd, g), ...);
}
constexpr int g_iss(int t, int NTILES, int PRE) {
  int i = 1;
  for (int u = -1; u <= t; ++u) { i += 2; if (i > u + 1 + PRE) i = u + 1 + PRE; if (i > NTILES - 1) i = NTILES - 1; }
  return i;
}
template <int NT, int KS, bool NATK, int NTILES, int D, int PRE, class EX, int T> DI void g_step_static(GemmC<NT, KS>& c, EX& ex) {
  constexpr int G = GemmC<NT, KS>::G, i0 = g_iss(T - 1, NTILES, PRE), i1 = g_iss(T, NTILES, PRE);
  constexpr bool done = (i0 == NTILES - 1), done_before = (g_iss(T - 2, NTILES, PRE) == NTILES - 1);
  constexpr int VM = (i0 - (T + 1)) * G + (done_before ? EX::N : 0);
  g_step_<NT, KS, NATK, D, (T + D) % D, (T == -1), (T == NTILES - 1), (i0 + 1) % D, i1 - i0, (done && !done_before), VM, EX>(c, ex, i0 + 1, make_iseq<(T == NTILES - 1) ? LA : NT * KS>{});
}
template <int NT, int KS, bool NATK, int NTILES, int D, int PRE, class EX, int T0, int... Ts> DI void g_tail(GemmC<NT, KS>& c, EX& ex, iseq<Ts...>) {
  (g_step_static<NT, KS, NATK, NTILES, D, PRE, EX, T0 + Ts>(c, ex), ...);
}
template <int NT, int KS, bool NATK, int D, int PRE, class EX, int... Ts> DI void g_body(GemmC<NT, KS>& c, EX& ex, int t, iseq<Ts...>) {
  (g_step_<NT, KS, NATK, D, Ts, false, false, (Ts + 1 + PRE) % D, 1, false, (PRE - 1) * GemmC<NT, KS>::G, EX>(c, ex, t + Ts + 1 + PRE, make_iseq<NT * KS>{}), ...);
}
template <int NT, int KS, bool NATK, int NTILES, int D, int PRE, class EX> DI void gemm_run(GemmC<NT, KS>& c, EX& ex) {
  static_assert(NTILES <= D || (PRE <= D - 2 && (D * NT * KS) % 8 == 0), "ring");
  static_assert(32 + D * KS * 4 <= 256 && 2 * NT * KS >= EX::N, "regs");
  TS(c.kid, 0);
  asm volatile("s_nop 4" ::: "memory");
  g_pre<NT, KS, D>(c, make_iseq<2 * GemmC<NT, KS>::G>{});
  if constexpr (NTILES <= D) g_tail<NT, KS, NATK, NTILES, D, PRE, EX, -1>(c, ex, make_iseq<NTILES + 1>{});
  else {
    constexpr int T0 = D * ((NTILES - 1 - PRE) / D);
    g_tail<NT, KS, NATK, NTILES, D, PRE, EX, -1>(c, ex, make_iseq<D + 1>{});
    for (int t = D; t < T0; t += D) g_body<NT, KS, NATK, D, PRE, EX>(c, ex, t, make_iseq<D>{});
    g_tail<NT, KS, NATK, NTILES, D, PRE, EX, T0>(c, ex, make_iseq<NTILES - T0>{});
  }
  asm volatile("s_waitcnt vmcnt(0)\n\ts_nop 7\n\ts_nop 7" ::: "memory");
#pragma unroll
  for (int nt = 0; nt < NT; ++nt) asm volatile("" : "+v"(c.acc[nt]));
  TS(c.kid, 2);
  GAP();
}
template <int NT, int KS> DI void set_vx(GemmC<NT, KS>& c, unsigned v) {
#pragma unroll
  for (int k = 0; k < KS; ++k) c.vx[k] = v + k * 65536u;
}
DI void bload16(u32x4& r, unsigned voff, rsrc_t srd, unsigned soff) { asm volatile("buffer_load_dwordx4 %0, %1, %2, %3 offen" : "=v"(r) : "v"(voff), "s"(srd), "s"(soff) : "memory"); }
template <int NT, int KS, bool NATK, bool SWAPN> DI void gemm_setup_rm(GemmC<NT, KS>& c, unsigned ldsb, unsigned lane, unsigned wid, unsigned ldB) {
  constexpr int P = GemmC<NT, KS>::P;
#pragma unroll
  for (int q = 0; q < P; ++q) { const unsigned p = wid * P + q, kg = p / (NT / 2), cp = p % (NT / 2);
    c.vw[q] = ldB * (8u * kg + ((lane >> 2) & 7u)) + 128u * cp + 64u * (lane >> 5) + 16u * (lane & 3u); }
  c.wstride = ldB * 16u * KS;
  c.m0w = rfl(ldsb + 1024u * P * wid);
  c.vtr = ldsb + tr_lane<NATK, SWAPN>(lane, NT * 512u);
}

DI float gelu_tanh(float v) {
  const float y2 = v * (-2.3022081985f - 0.1029432397f * v * v);
  return v * __builtin_amdgcn_rcpf(1.f + __builtin_amdgcn_exp2f(y2));
}
__global__ __launch_bounds__(256, 1) void k_zgemm(const unsigned char* __restrict__ xnF, const unsigned short* __restrict__ w_in, unsigned char* __restrict__ zF) {
  extern __shared__ __attribute__((aligned(16))) char lds[];
  const unsigned lane = threadIdx.x & 63u, wid = rfl(threadIdx.x >> 6);
  const unsigned xcd = blockIdx.x & 7u, idx = blockIdx.x >> 3;
  const unsigned rb128 = 4u * (xcd >> 1) + idx / 7u, cb = 7u * (xcd & 1u) + idx % 7u, rb = 4u * rb128 + wid;
  GemmC<4, 5> c; c.kid = 1;
  gemm_setup_rm<4, 5, true, false>(c, (unsigned)(size_t)lds, lane, wid, FF * 2u);
  c.srdW = mk_srd((const char*)w_in + cb * 256u, DM * FF * 2u - cb * 256u);
  c.srdX = mk_srd(xnF, SEQ * DM * 2u);
  set_vx(c, rb * 1024u + lane * 16u);
  NoEx ex; gemm_run<4, 5, true, 8, 8, 6>(c, ex);
#pragma unroll
  for (int nt = 0; nt < 4; ++nt) {
    const unsigned ct = 4u * cb + nt;
    if (ct < 54u) {
#pragma unroll
      for (int a = 0; a < 2; ++a) {
        u32x4 w;
#pragma unroll
        for (int e = 0; e < 4; ++e) w[e] = cvtpk(gelu_tanh(c.acc[nt][8 * a + 2 * e]), gelu_tanh(c.acc[nt][8 * a + 2 * e + 1]));
        *(u32x4*)(zF + ((size_t)((2u * ct + a) * 64u + rb) * 64u + lane) * 16u) = w;
      }
    }
  }
}

template <int NT> struct ExZ { static constexpr int N = 2 * NT; u32x4 r[N]; unsigned voff; rsrc_t srd; template <int J> DI void issue() { bload16(r[J], voff, srd, J * 65536u); } };
template <int NT> DI void spatial_unit(char* lds, unsigned lane, unsigned wid, unsigned rb, unsigned ct0, const unsigned char* wspF, const unsigned char* z2nT,
                                       const unsigned char* zF, const unsigned short* b_sp, unsigned char* uF) {
  GemmC<NT, 4> c; c.kid = 3;
  const unsigned ldsb = (unsigned)(size_t)lds;
#pragma unroll
  for (int q = 0; q < NT; ++q) { const unsigned sg = 2u * (wid * NT + q) + (lane >> 5), kg = sg / NT, nt = sg % NT;
    c.vw[q] = (kg * 27u + ct0 + nt) * 512u + (lane & 31u) * 16u; }
  c.wstride = 8u * 27u * 512u;
  c.m0w = rfl(ldsb + 1024u * NT * wid);
  c.vtr = ldsb + tr_lane<true, true>(lane, NT * 512u);
  c.srdW = mk_srd(z2nT, SEQ * FH * 2u);
  c.srdX = mk_srd(wspF, SEQ * SEQ * 2u);
  set_vx(c, rb * 1024u + lane * 16u);
  const float bias = bflo((unsigned)b_sp[rb * 32u + (lane & 31u)]);
  ExZ<NT> ex; ex.voff = (2u * ct0 * 64u + rb) * 1024u + lane * 16u; ex.srd = mk_srd(zF, SEQ * FF * 2u);
  asm volatile("s_waitcnt vmcnt(0)" :: "v"(bias) : "memory");
  gemm_run<NT, 4, true, 32, 8, 6>(c, ex);
#pragma unroll
  for (int j = 0; j < 2 * NT; ++j) asm volatile("" : "+v"(ex.r[j]));
#pragma unroll
  for (int nt = 0; nt < NT; ++nt)
#pragma unroll
    for (int a = 0; a < 2; ++a) {
      const unsigned ks = 2u * (ct0 + nt) + a;
      const u32x4 z1 = ex.r[2 * nt + a];
      u32x4 w;
#pragma unroll
      for (int e = 0; e < 4; ++e) w[e] = cvtpk(bflo(z1[e]) * (c.acc[nt][8 * a + 2 * e] + bias), bfhi(z1[e]) * (c.acc[nt][8 * a + 2 * e + 1] + bias));
      *(u32x4*)(uF + ((size_t)(ks * 64u + rb) * 64u + lane) * 16u) = w;
    }
}
__global__ __launch_bounds__(256, 1) void k_mid(const unsigned char* __restrict__ OF, const unsigned short* __restrict__ wo, unsigned char* __restrict__ sP,
                                                const unsigned char* __restrict__ wspF, const unsigned char* __restrict__ z2nT, const unsigned char* __restrict__ zF,
                                                const unsigned short* __restrict__ b_sp, unsigned char* __restrict__ uF) {
  extern __shared__ __attribute__((aligned(16))) char lds[];
  const unsigned lane = threadIdx.x & 63u, wid = rfl(threadIdx.x >> 6);
  const unsigned xcd = blockIdx.x & 7u, idx = blockIdx.x >> 3;
  if (idx >= 12u) {
    const unsigned i = idx - 12u, kh = xcd & 1u, rb128 = 4u * (xcd >> 1) + i / 5u, cb = i % 5u, rb = 4u * rb128 + wid;
    GemmC<4, 4> c; c.kid = 3;
    gemm_setup_rm<4, 4, false, true>(c, (unsigned)(size_t)lds, lane, wid, DM * 2u);
    const unsigned wofs = kh * 2048u * DM * 2u + cb * 256u;
    c.srdW = mk_srd((const char*)wo + wofs, NH * DH * DM * 2u - wofs);
    c.srdX = mk_srd(OF, SEQ * NH * DH * 2u);
    set_vx(c, (kh * 128u * 64u + rb) * 1024u + lane * 16u);
    NoEx ex; gemm_run<4, 4, false, 32, 8, 6>(c, ex);
#pragma unroll
    for (int nt = 0; nt < 4; ++nt)
#pragma unroll
      for (int g = 0; g < 4; ++g) {
        f32x4 w = {c.acc[nt][4 * g], c.acc[nt][4 * g + 1], c.acc[nt][4 * g + 2], c.acc[nt][4 * g + 3]};
        *(f32x4*)(sP + ((size_t)(((kh * 64u + rb) * 20u + 4u * cb + nt) * 4u + g) * 64u + lane) * 16u) = w;
      }
  } else {
    const unsigned rb128 = 4u * (xcd >> 1) + idx / 3u, j = 3u * (xcd & 1u) + idx % 3u, rb = 4u * rb128 + wid;
    if (j & 1u) spatial_unit<4>(lds, lane, wid, rb, 15u + 4u * (j >> 1), wspF, z2nT, zF, b_sp, uF);
    else        spatial_unit<5>(lds, lane, wid, rb, 5u * (j >> 1), wspF, z2nT, zF, b_sp, uF);
  }
}

struct ExO { static constexpr int N = 20; u32x4 r[N]; unsigned vs, vxx; rsrc_t srdS, srdXx;
  template <int J> DI void issue() { if constexpr (J < 16) bload16(r[J], vs, srdS, (J >> 3) * (64u * 20u * 4096u) + (J & 7) * 1024u); else bload16(r[J], vxx, srdXx, (J - 16) * 32u); } };
__global__ __launch_bounds__(256, 1) void k_out(const unsigned char* __restrict__ uF, const unsigned short* __restrict__ w_out, const unsigned char* __restrict__ sP,
                                                const unsigned short* __restrict__ x, unsigned short* __restrict__ out) {
  extern __shared__ __attribute__((aligned(16))) char lds[];
  const unsigned lane = threadIdx.x & 63u, wid = rfl(threadIdx.x >> 6);
  const unsigned xcd = blockIdx.x & 7u, idx = blockIdx.x >> 3;
  const unsigned rb128 = 4u * (xcd >> 1) + idx / 5u, cb = 5u * (xcd & 1u) + idx % 5u, rb = 4u * rb128 + wid;
  GemmC<2, 6> c; c.kid = 4;
  gemm_setup_rm<2, 6, false, true>(c, (unsigned)(size_t)lds, lane, wid, DM * 2u);
  c.srdW = mk_srd((const char*)w_out + cb * 128u, FH * DM * 2u - cb * 128u);
  c.srdX = mk_srd(uF, SEQ * FH * 2u);
  set_vx(c, rb * 1024u + lane * 16u);
  const unsigned row = rb * 32u + (lane & 31u), hi = lane >> 5;
  ExO ex; ex.vs = (rb * 20u + 2u * cb) * 4096u + lane * 16u; ex.vxx = row * 1280u + cb * 128u + hi * 16u;
  ex.srdS = mk_srd(sP, 2u * SEQ * DM * 4u); ex.srdXx = mk_srd(x, SEQ * DM * 2u);
  gemm_run<2, 6, false, 9, 9, 6>(c, ex);
#pragma unroll
  for (int j = 0; j < 20; ++j) asm volatile("" : "+v"(ex.r[j]));
#pragma unroll
  for (int nt = 0; nt < 2; ++nt) {
    const unsigned ct = 2u * cb + nt;
#pragma unroll
    for (int a = 0; a < 2; ++a) {
      const size_t eo = (size_t)row * DM + 32u * ct + 16u * a + 8u * hi;
      const u32x4 xv = ex.r[16 + 2 * nt + a];
      float sv[8];
#pragma unroll
      for (int g = 0; g < 2; ++g) {
        const f32x4 p0 = __builtin_bit_cast(f32x4, ex.r[4 * nt + 2 * a + g]), p1 = __builtin_bit_cast(f32x4, ex.r[8 + 4 * nt + 2 * a + g]);
#pragma unroll
        for (int e = 0; e < 4; ++e) sv[4 * g + e] = p0[e] + p1[e];
      }
      u32x4 w;
#pragma unroll
      for (int e = 0; e < 4; ++e) {
        const float b0 = c.acc[nt][8 * a + 2 * e], b1 = c.acc[nt][8 * a + 2 * e + 1];
        const float g0 = b0 * __builtin_amdgcn_rcpf(1.f + __builtin_amdgcn_exp2f(-1.4426950408889634f * b0));
        const float g1 = b1 * __builtin_amdgcn_rcpf(1.f + __builtin_amdgcn_exp2f(-1.4426950408889634f * b1));
        w[e] = cvtpk(bflo(xv[e]) + sv[2 * e] * g0, bfhi(xv[e]) + sv[2 * e + 1] * g1);
      }
      *(u32x4*)(out + eo) = w;
    }
  }
}

constexpr int NTILE = SEQ / 64;
constexpr unsigned KRS = 256u, KTILEB = 64u * KRS;
constexpr float THR = 8.0f;
#define KRING 0x0000u
#define VRING 0x8000u
#define WRING 0x0u
DI float w4_aone() { unsigned l; asm volatile("v_mbcnt_lo_u32_b32 %0, -1, 0\n\tv_mbcnt_hi_u32_b32 %0, -1, %0" : "=v"(l)); return l < 32u ? 1.f : 0.f; }
DI void negm_bcast(f32x16& negm, float aone, float neg_mhat) { asm volatile("s_nop 1\n\tv_mfma_f32_32x32x2_f32 %0, %1, %2, 0" : "+v"(negm) : "v"(aone), "v"(neg_mhat)); }
struct W4C {
  unsigned vkb[4], vvb, vksrc[2], vvsrc, m0k, m0v, sofk, sofv, ldsb;
  rsrc_t srdK, srdV;
  f32x16 S[2][2][2];
  f32x16 NEGM[2];
  bf16x8 Vf[4][4];
  bf16x8 Pk[2][4];
  bf16x4 vlo[4][4], vup[4][4];
  float mhat[2], lsum[2], fct[2]; bool pend;
  float rs0[2], rs1[2], pmx0[2], pmx1[2], rmax[2], delta[2];
  unsigned pm0[8], psf[8];
  f32x2 rs2[2];
  u32x4 sw; unsigned vsl, vss, ssl, sss; rsrc_t srdSL, srdSS;
};
constexpr int AO_(int db, int qb) { return 16 * (2 * db + qb); }
constexpr int AQ_(int qb, int d0) { return 128 + 32 * qb + 4 * d0; }
constexpr int AK_(int kb, int d0) { return 192 + 32 * kb + 4 * d0; }
template <int I> DI void tr_read(W4C& c, unsigned vslot) {
  constexpr int ks = I >> 3, db = (I >> 1) & 3;
  if constexpr ((I & 1) == 0) asm volatile("ds_read_b64_tr_b16 %0, %1 offset:%2" : "=v"(c.vlo[db][ks]) : "v"(c.vvb + vslot), "i"(4096 * ks + 512 * db) : "memory");
  else                        asm volatile("ds_read_b64_tr_b16 %0, %1 offset:%2" : "=v"(c.vup[db][ks]) : "v"(c.vvb + vslot), "i"(4096 * ks + 2048 + 512 * db) : "memory");
}
template <int I> DI void k_read(W4C& c, unsigned slot) {
  constexpr int kb = I >> 3, d0 = I & 7;
  asm volatile("ds_read_b128 a[%c1:%c2], %0 offset:%c3" :: "v"(c.vkb[d0 & 3] + slot), "i"(AK_(kb, d0)), "i"(AK_(kb, d0) + 3), "i"(8192 * kb + 128 * (d0 >> 2)) : "memory", ACLOB_HI);
}
template <int X, bool FIRST, int M> DI void qk_mfma(W4C& c) {
  constexpr int d0 = M >> 2, kb = (M >> 1) & 1, qb = M & 1, ak = AK_(kb, d0), aq = AQ_(qb, d0);
  if constexpr (d0 == 0) {
    if constexpr (FIRST) asm volatile("v_mfma_f32_32x32x16_bf16 %0, a[%c1:%c2], a[%c3:%c4], 0" : "=&v"(c.S[X][kb][qb]) : "i"(ak), "i"(ak + 3), "i"(aq), "i"(aq + 3) : ACLOB);
    else                 asm volatile("v_mfma_f32_32x32x16_bf16 %0, a[%c2:%c3], a[%c4:%c5], %1" : "=&v"(c.S[X][kb][qb]) : "v"(c.NEGM[qb]), "i"(ak), "i"(ak + 3), "i"(aq), "i"(aq + 3) : ACLOB);
  } else asm volatile("v_mfma_f32_32x32x16_bf16 %0, a[%c1:%c2], a[%c3:%c4], %0" : "+v"(c.S[X][kb][qb]) : "i"(ak), "i"(ak + 3), "i"(aq), "i"(aq + 3) : ACLOB);
}
template <int M, bool ZERO> DI void pv_mfma(W4C& c) {
  constexpr int ks = M >> 3, db = (M >> 1) & 3, qb = M & 1, ao = AO_(db, qb);
  if constexpr (ZERO && ks == 0) asm volatile("v_mfma_f32_32x32x16_bf16 a[%c2:%c3], %0, %1, 0" :: "v"(c.Vf[db][ks]), "v"(c.Pk[qb][ks]), "i"(ao), "i"(ao + 15) : ACLOB_LO);
  else                         asm volatile("v_mfma_f32_32x32x16_bf16 a[%c2:%c3], %0, %1, a[%c2:%c3]" :: "v"(c.Vf[db][ks]), "v"(c.Pk[qb][ks]), "i"(ao), "i"(ao + 15) : ACLOB_LO);
}
template <int X, int QB, int KS, int P> DI void pack1(W4C& c) {
  u32x4 w = __builtin_bit_cast(u32x4, c.Pk[QB][KS]);
  w[P] = cvtpk(c.S[X][KS >> 1][QB][8 * (KS & 1) + 2 * P], c.S[X][KS >> 1][QB][8 * (KS & 1) + 2 * P + 1]);
  c.Pk[QB][KS] = __builtin_bit_cast(bf16x8, w);
}
template <int X, int J> DI void rowsum1(W4C& c) {
  constexpr int qb = J >> 4, ks = (J >> 2) & 3, p = J & 3;
  c.rs0[qb] = fadd_s(c.rs0[qb], c.S[X][ks >> 1][qb][8 * (ks & 1) + 2 * p]); c.rs1[qb] = fadd_s(c.rs1[qb], c.S[X][ks >> 1][qb][8 * (ks & 1) + 2 * p + 1]);
}
template <int X, bool FIRST, int M> DI void gapA(W4C& c, unsigned vslot, const int t) {
  constexpr int Xp = X ^ 1;
  qk_mfma<X, FIRST, M>(c);
  if constexpr (M >= 16 && M <= 31) {
    constexpr int q = (M - 16) >> 1;
    if constexpr (((M - 16) & 1) == 0) {
      if constexpr (q < 4) c.pm0[q] = c.m0k + (unsigned)((t + 2) & 1) * 0x4000u + 1024u * q; else c.pm0[q] = c.m0v + (unsigned)((t + 1) & 1) * 0x4000u + 1024u * (q - 4);
      asm volatile("" : "+s"(c.pm0[q]));
    } else {
      if constexpr (q < 4) c.psf[q] = KTILEB * (unsigned)(t + 2) + c.sofk + KRS * 4u * q; else c.psf[q] = KTILEB * (unsigned)(t + 1) + c.sofv + KRS * 8u * ((q - 4) >> 1) + 128u * ((q - 4) & 1);
      asm volatile("" : "+s"(c.psf[q]));
    }
  }
  if constexpr (M == 31) asm volatile("s_nop 4" : "+s"(c.pm0[0]), "+s"(c.pm0[1]), "+s"(c.pm0[2]), "+s"(c.pm0[3]), "+s"(c.pm0[4]), "+s"(c.pm0[5]), "+s"(c.pm0[6]), "+s"(c.pm0[7]),
                                                  "+s"(c.psf[0]), "+s"(c.psf[1]), "+s"(c.psf[2]), "+s"(c.psf[3]), "+s"(c.psf[4]), "+s"(c.psf[5]), "+s"(c.psf[6]), "+s"(c.psf[7]));
  if constexpr (!FIRST && M == 8) asm volatile("" :: "v"(c.NEGM[0]), "v"(c.NEGM[1]));
  if constexpr (!FIRST) {
    constexpr int base = M + (M >= 4) + (M >= 8) + (M >= 12) + (M >= 16) + (M >= 20) + (M >= 24);
    if constexpr (M <= 25) tr_read<base>(c, vslot);
    if constexpr (M == 3 || M == 7 || M == 11 || M == 15 || M == 19 || M == 23) tr_read<base + 1>(c, vslot);
    if constexpr (M < 16) {
      constexpr int e0 = 2 * M, e1 = 2 * M + 1;
      c.S[Xp][1][e0 >> 4][e0 & 15] = __builtin_amdgcn_exp2f(c.S[Xp][1][e0 >> 4][e0 & 15]);
      c.S[Xp][1][e1 >> 4][e1 & 15] = __builtin_amdgcn_exp2f(c.S[Xp][1][e1 >> 4][e1 & 15]);
      pack1<Xp, (M >> 3), ((M >> 2) & 1), (M & 3)>(c);
    } else {
      constexpr int cc = M - 16;
      pack1<Xp, (cc >> 3), 2 + ((cc >> 2) & 1), (cc & 3)>(c);
      rowsum1<Xp, 16 * (cc >> 3) + 8 + 4 * ((cc >> 2) & 1) + (cc & 3)>(c);
    }
  }
  GAP();
}
#define W4_VLO "+v"(c.vlo[0][0]), "+v"(c.vlo[0][1]), "+v"(c.vlo[0][2]), "+v"(c.vlo[0][3]), "+v"(c.vlo[1][0]), "+v"(c.vlo[1][1]), "+v"(c.vlo[1][2]), "+v"(c.vlo[1][3]), \
               "+v"(c.vlo[2][0]), "+v"(c.vlo[2][1]), "+v"(c.vlo[2][2]), "+v"(c.vlo[2][3]), "+v"(c.vlo[3][0]), "+v"(c.vlo[3][1]), "+v"(c.vlo[3][2]), "+v"(c.vlo[3][3])
#define W4_VUP "+v"(c.vup[0][0]), "+v"(c.vup[0][1]), "+v"(c.vup[0][2]), "+v"(c.vup[0][3]), "+v"(c.vup[1][0]), "+v"(c.vup[1][1]), "+v"(c.vup[1][2]), "+v"(c.vup[1][3]), \
               "+v"(c.vup[2][0]), "+v"(c.vup[2][1]), "+v"(c.vup[2][2]), "+v"(c.vup[2][3]), "+v"(c.vup[3][0]), "+v"(c.vup[3][1]), "+v"(c.vup[3][2]), "+v"(c.vup[3][3])
DI void v_concat(W4C& c) {
#pragma unroll
  for (int ks = 0; ks < 4; ++ks)
#pragma unroll
    for (int db = 0; db < 4; ++db) c.Vf[db][ks] = __builtin_shufflevector(c.vlo[db][ks], c.vup[db][ks], 0, 1, 2, 3, 4, 5, 6, 7);
}
DI void lsum_merge(W4C& c) {
#pragma unroll
  for (int qb = 0; qb < 2; ++qb) {
    const float rs = fadd_s(c.rs0[qb], c.rs1[qb]);
    auto pr = __builtin_amdgcn_permlane32_swap(__float_as_uint(rs), __float_as_uint(rs), false, false);
    c.lsum[qb] = fadd_s(c.lsum[qb], fadd_s(__uint_as_float(pr[0]), __uint_as_float(pr[1])));
  }
}
template <int X, bool FIRST, int VMA, int... Ms> DI void phaseA_(W4C& c, const int t, iseq<Ms...>) {
  const unsigned vslot = (unsigned)((t - 1) & 1) * 0x4000u;
  (gapA<X, FIRST, Ms>(c, vslot, t), ...);
  if constexpr (!FIRST) lsum_merge(c);
  if constexpr (FIRST) asm volatile("s_waitcnt vmcnt(0) lgkmcnt(0)\n\ts_barrier" ::: "memory");
  else { asm volatile("s_waitcnt vmcnt(%c16) lgkmcnt(0)" : W4_VLO : "i"(VMA) : "memory"); asm volatile("s_barrier" : W4_VUP :: "memory"); }
  GAP();
  if constexpr (!FIRST) v_concat(c);
}
template <int X, bool FIRST, int VMA = 0> DI void phaseA(W4C& c, const int t) { phaseA_<X, FIRST, VMA>(c, t, make_iseq<32>{}); }

template <int X, bool PEEL, bool ZERO1, bool DK, bool DV, bool KR, int SJ, int M> DI void gapB(W4C& c, const unsigned kslot_rd) {
  if constexpr (!PEEL) pv_mfma<M, ZERO1>(c);
  if constexpr (M < 4 && DK) dma16(c.pm0[M], c.vksrc[M & 1], c.srdK, c.psf[M]);
  else if constexpr (M >= 4 && M < 8 && DV) dma16(c.pm0[M], c.vvsrc, c.srdV, c.psf[M]);
  if constexpr ((SJ & 1) && M == 17) asm volatile("buffer_store_dwordx4 %0, %1, %2, %3 offen\n\ts_nop 1" : "+v"(c.sw) : "v"(c.vss), "s"(c.srdSS), "s"(c.sss) : "memory");
  if constexpr ((SJ & 2) && M == 18) asm volatile("buffer_load_dwordx4 %0, %1, %2, %3 offen" : "=v"(c.sw) : "v"(c.vsl), "s"(c.srdSL), "s"(c.ssl) : "memory");
  if constexpr (M >= 1 && M <= 16 && KR) k_read<M - 1>(c, kslot_rd);
  if constexpr (M >= 8 && M < 16) {
    constexpr int i = M - 8, q = i >> 2, r = 4 * (i & 3);
    if constexpr ((i & 3) == 0) { c.pmx0[q] = max3f(c.S[X][0][q][0], c.S[X][0][q][1], c.S[X][1][q][0]); c.pmx1[q] = max3f(c.S[X][0][q][2], c.S[X][0][q][3], c.S[X][1][q][1]);
                                  c.pmx0[q] = max3f(c.pmx0[q], c.S[X][1][q][2], c.S[X][1][q][3]); }
    else                        { c.pmx0[q] = max3f(c.pmx0[q], c.S[X][0][q][r], c.S[X][0][q][r + 1]); c.pmx1[q] = max3f(c.pmx1[q], c.S[X][0][q][r + 2], c.S[X][0][q][r + 3]);
                                  c.pmx0[q] = max3f(c.pmx0[q], c.S[X][1][q][r], c.S[X][1][q][r + 1]); c.pmx1[q] = max3f(c.pmx1[q], c.S[X][1][q][r + 2], c.S[X][1][q][r + 3]); }
  }
  if constexpr (M == 16 || M == 17) {
    constexpr int q = M - 16; const float mm = max2f(c.pmx0[q], c.pmx1[q]);
    auto pr = __builtin_amdgcn_permlane32_swap(__float_as_uint(mm), __float_as_uint(mm), false, false);
    c.rmax[q] = max2f(__uint_as_float(pr[0]), __uint_as_float(pr[1]));
  }
  if constexpr (M == 18) {
    if constexpr (PEEL) {
#pragma unroll
      for (int q = 0; q < 2; ++q) { c.mhat[q] = c.rmax[q]; c.lsum[q] = 0.f; c.fct[q] = 1.f;
#pragma unroll
        for (int kb = 0; kb < 2; ++kb) { _Pragma("unroll") for (int e = 0; e < 16; ++e) c.S[X][kb][q][e] = fsub_s(c.S[X][kb][q][e], c.rmax[q]); }
        negm_bcast(c.NEGM[q], w4_aone(), -c.mhat[q]);
      }
    } else {
      const bool grew = (c.rmax[0] > THR) | (c.rmax[1] > THR);
      if (__builtin_expect(__any((int)grew), 0)) {
#pragma unroll
        for (int q = 0; q < 2; ++q) { const float dl = max2f(c.rmax[q], 0.f); c.delta[q] = dl; c.mhat[q] = fadd_s(c.mhat[q], dl); c.fct[q] = __builtin_amdgcn_exp2f(-dl);
#pragma unroll
          for (int kb = 0; kb < 2; ++kb) { _Pragma("unroll") for (int e = 0; e < 16; ++e) c.S[X][kb][q][e] = fsub_s(c.S[X][kb][q][e], dl); }
          negm_bcast(c.NEGM[q], w4_aone(), -c.mhat[q]);
        }
        c.pend = true;
      }
    }
  }
  if constexpr (M == 18) { c.rs0[0] = c.rs0[1] = c.rs1[0] = c.rs1[1] = 0.f; }
  if constexpr (M >= 19) {
    constexpr int g = M - 19, e_lo = (32 * g) / 13, e_hi = (32 * (g + 1)) / 13;
    {
      constexpr int p_lo = g ? (32 * (g - 1)) / 13 : 0, p_hi = g ? (32 * g) / 13 : 0;
      if constexpr (p_lo + 0 < p_hi) { constexpr int e = p_lo + 0; if constexpr (e & 1) c.rs1[e >> 4] = fadd_s(c.rs1[e >> 4], c.S[X][0][e >> 4][e & 15]); else c.rs0[e >> 4] = fadd_s(c.rs0[e >> 4], c.S[X][0][e >> 4][e & 15]); }
      if constexpr (p_lo + 1 < p_hi) { constexpr int e = p_lo + 1; if constexpr (e & 1) c.rs1[e >> 4] = fadd_s(c.rs1[e >> 4], c.S[X][0][e >> 4][e & 15]); else c.rs0[e >> 4] = fadd_s(c.rs0[e >> 4], c.S[X][0][e >> 4][e & 15]); }
      if constexpr (p_lo + 2 < p_hi) { constexpr int e = p_lo + 2; if constexpr (e & 1) c.rs1[e >> 4] = fadd_s(c.rs1[e >> 4], c.S[X][0][e >> 4][e & 15]); else c.rs0[e >> 4] = fadd_s(c.rs0[e >> 4], c.S[X][0][e >> 4][e & 15]); }
    }
    if constexpr (e_lo + 0 < e_hi) c.S[X][0][(e_lo + 0) >> 4][(e_lo + 0) & 15] = __builtin_amdgcn_exp2f(c.S[X][0][(e_lo + 0) >> 4][(e_lo + 0) & 15]);
    if constexpr (e_lo + 1 < e_hi) c.S[X][0][(e_lo + 1) >> 4][(e_lo + 1) & 15] = __builtin_amdgcn_exp2f(c.S[X][0][(e_lo + 1) >> 4][(e_lo + 1) & 15]);
    if constexpr (e_lo + 2 < e_hi) c.S[X][0][(e_lo + 2) >> 4][(e_lo + 2) & 15] = __builtin_amdgcn_exp2f(c.S[X][0][(e_lo + 2) >> 4][(e_lo + 2) & 15]);
  }
  if constexpr (M == 31) asm volatile("" :: "v"(c.S[X][0][0]), "v"(c.S[X][0][1]), "v"(c.NEGM[0]), "v"(c.NEGM[1]), "v"(c.rs0[0]), "v"(c.rs0[1]), "v"(c.rs1[0]), "v"(c.rs1[1]));
  GAP();
}
template <int R> DI void o_scale1(float f0, float f1) {
  float t; asm volatile("v_accvgpr_read_b32 %0, a[%c2]\n\tv_mul_f32 %0, %1, %0\n\tv_accvgpr_write_b32 a[%c2], %0" : "=&v"(t) : "v"(((R >> 4) & 1) ? f1 : f0), "i"(R) : ACLOB_LO);
}
template <int... Rs> DI void o_scale(float f0, float f1, iseq<Rs...>) { (o_scale1<Rs>(f0, f1), ...); }
template <int X, bool PEEL, bool ZERO1, bool DK, bool DV, bool KR, int SJ, int... Ms> DI void phaseB_(W4C& c, const int t, iseq<Ms...>) {
  const unsigned kslot_rd = (unsigned)((t + 1) & 1) * 0x4000u;
  if constexpr (!PEEL) asm volatile("s_nop 1" ::: "memory");
  GAP();
  (gapB<X, PEEL, ZERO1, DK, DV, KR, SJ, Ms>(c, kslot_rd), ...);
#pragma unroll
  for (int e = (32 * 12) / 13; e < 32; ++e) { if (e & 1) c.rs1[e >> 4] = fadd_s(c.rs1[e >> 4], c.S[X][0][e >> 4][e & 15]); else c.rs0[e >> 4] = fadd_s(c.rs0[e >> 4], c.S[X][0][e >> 4][e & 15]); }
  if constexpr (!PEEL) {
    if (__builtin_expect(c.pend, 0)) {
      asm volatile("s_nop 7\n\ts_nop 7" ::: "memory");
      o_scale(c.fct[0], c.fct[1], make_iseq<128>{});
      asm volatile("s_nop 1" ::: "memory");
      c.lsum[0] = fmul_s(c.lsum[0], c.fct[0]); c.lsum[1] = fmul_s(c.lsum[1], c.fct[1]); c.pend = false;
    }
  }
  asm volatile("s_waitcnt lgkmcnt(0)" ::: "memory");
  GAP();
}
template <int X, bool PEEL, bool ZERO1, bool DK = true, bool DV = true, bool KR = true, int SJ = 0> DI void phaseB(W4C& c, const int t) { phaseB_<X, PEEL, ZERO1, DK, DV, KR, SJ>(c, t, make_iseq<32>{}); }
template <int... Is> DI void tr_reads(W4C& c, unsigned vslot, iseq<Is...>) { (tr_read<Is>(c, vslot), ...); }
template <int X, int... Cs> DI void packs(W4C& c, iseq<Cs...>) { (pack1<X, ((Cs >> 3) & 1), 2 * (Cs >> 4) + ((Cs >> 2) & 1), (Cs & 3)>(c), ...); }
template <int X, int J> DI void rowsum1_padded(W4C& c) { rowsum1<X, J>(c); asm volatile("s_nop 1"); }
template <int X, int... Js> DI void rowsums(W4C& c, iseq<Js...>) { (rowsum1_padded<X, Js>(c), ...); }
template <int... Ms> DI void pv_all(W4C& c, iseq<Ms...>) { (pv_mfma<Ms, false>(c), ...); }

template <int X, int KB, int E> DI void f_exp(W4C& c) { constexpr int qb = KB ? (E >> 4) : ((E >> 3) & 1), r = KB ? (E & 15) : 8 * (E >> 4) + (E & 7); c.S[X][KB][qb][r] = __builtin_amdgcn_exp2f(c.S[X][KB][qb][r]); }
template <int X, int KB, int I> DI void f_add(W4C& c) { constexpr int qb = KB ? (I >> 3) : ((I >> 2) & 1), r = KB ? 2 * (I & 7) : 8 * (I >> 3) + 2 * (I & 3);
  const f32x2 p = __builtin_shufflevector(c.S[X][KB][qb], c.S[X][KB][qb], r, r + 1); asm("v_pk_add_f32 %0, %0, %1" : "+v"(c.rs2[qb]) : "v"(p)); }
template <int X, int KB, int I> DI void f_pack(W4C& c) { pack1<X, (KB ? (I >> 3) : ((I >> 2) & 1)), 2 * KB + (KB ? ((I >> 2) & 1) : (I >> 3)), (I & 3)>(c); }
template <int X, int M> DI void gapAf(W4C& c, unsigned vslot, const int t) {
  constexpr int Xp = X ^ 1;
  qk_mfma<X, false, M>(c);
  if constexpr (M >= 16) {
    constexpr int q = (M - 16) >> 1;
    if constexpr (((M - 16) & 1) == 0) {
      if constexpr (q < 4) c.pm0[q] = c.m0k + (unsigned)((t + 2) & 1) * 0x4000u + 1024u * q; else c.pm0[q] = c.m0v + (unsigned)((t + 1) & 1) * 0x4000u + 1024u * (q - 4);
      asm volatile("" : "+s"(c.pm0[q]));
    } else {
      if constexpr (q < 4) c.psf[q] = KTILEB * (unsigned)(t + 2) + c.sofk + KRS * 4u * q; else c.psf[q] = KTILEB * (unsigned)(t + 1) + c.sofv + KRS * 8u * ((q - 4) >> 1) + 128u * ((q - 4) & 1);
      asm volatile("" : "+s"(c.psf[q]));
    }
  }
  if constexpr (M == 8) asm volatile("" :: "v"(c.NEGM[0]), "v"(c.NEGM[1]));
  if constexpr (M < 12) { tr_read<(3 * M) / 2>(c, vslot); if constexpr (M & 1) tr_read<(3 * M) / 2 + 1>(c, vslot); }
  else if constexpr (M < 26) tr_read<M + 6>(c, vslot);
  if constexpr (M == 0) { f_add<Xp, 0, 15>(c); f_pack<Xp, 0, 15>(c); }
  f_exp<Xp, 1, M>(c);
  if constexpr (M >= 2 && (M & 1) == 0) f_add<Xp, 1, (M - 2) / 2>(c);
  if constexpr (M >= 3 && (M & 1) == 1) f_pack<Xp, 1, (M - 3) / 2>(c);
  GAP();
}
template <int X, int VMA, int... Ms> DI void phaseAf_(W4C& c, const int t, iseq<Ms...>) {
  const unsigned vslot = (unsigned)((t - 1) & 1) * 0x4000u;
  (gapAf<X, Ms>(c, vslot, t), ...);
  asm volatile("s_waitcnt vmcnt(%c16) lgkmcnt(0)" : W4_VLO : "i"(VMA) : "memory"); asm volatile("s_barrier" : W4_VUP :: "memory");
  GAP();
  v_concat(c);
}
template <int X, int VMA = 0> DI void phaseAf(W4C& c, const int t) { phaseAf_<X, VMA>(c, t, make_iseq<32>{}); }
template <int X, bool PEEL, bool ZERO1, bool DK, bool DV, bool KR, int SJ, int M> DI void gapBf(W4C& c, const unsigned kslot_rd) {
  if constexpr (!PEEL) pv_mfma<M, ZERO1>(c);
  if constexpr (M < 4 && DK) dma16(c.pm0[M], c.vksrc[M & 1], c.srdK, c.psf[M]);
  else if constexpr (M >= 4 && M < 8 && DV) dma16(c.pm0[M], c.vvsrc, c.srdV, c.psf[M]);
  if constexpr ((SJ & 1) && M == 25) asm volatile("buffer_store_dwordx4 %0, %1, %2, %3 offen\n\ts_nop 1" : "+v"(c.sw) : "v"(c.vss), "s"(c.srdSS), "s"(c.sss) : "memory");
  if constexpr ((SJ & 2) && M == 27) asm volatile("buffer_load_dwordx4 %0, %1, %2, %3 offen" : "=v"(c.sw) : "v"(c.vsl), "s"(c.srdSL), "s"(c.ssl) : "memory");
  if constexpr (M >= 8 && M < 24 && KR) k_read<M - 8>(c, kslot_rd);
  if constexpr (!PEEL && M == 9) { f_add<X ^ 1, 1, 15>(c); f_pack<X ^ 1, 1, 15>(c); }
  f_exp<X, 0, M>(c);
  if constexpr (M >= 2 && (M & 1) == 0) f_add<X, 0, (M - 2) / 2>(c);
  if constexpr (M >= 17) f_pack<X, 0, M - 17>(c);
  GAP();
}
template <int X, bool PEEL, bool ZERO1, bool DK, bool DV, bool KR, int SJ, int... Ms> DI void phaseBf_(W4C& c, const int t, iseq<Ms...>) {
  const unsigned kslot_rd = (unsigned)((t + 1) & 1) * 0x4000u;
  if constexpr (PEEL) {
    asm volatile("s_nop 7\n\ts_nop 7" ::: "memory");
#pragma unroll
    for (int q = 0; q < 2; ++q) {
      float m0 = max2f(c.S[X][0][q][0], c.S[X][1][q][0]);
#pragma unroll
      for (int e = 1; e < 16; ++e) m0 = max3f(m0, c.S[X][0][q][e], c.S[X][1][q][e]);
      auto pr = __builtin_amdgcn_permlane32_swap(__float_as_uint(m0), __float_as_uint(m0), false, false);
      const float mx = max2f(__uint_as_float(pr[0]), __uint_as_float(pr[1]));
#pragma unroll
      for (int kb = 0; kb < 2; ++kb) { _Pragma("unroll") for (int e = 0; e < 16; ++e) c.S[X][kb][q][e] = fsub_s(c.S[X][kb][q][e], mx); }
      negm_bcast(c.NEGM[q], w4_aone(), -mx);
      c.rs2[q] = (f32x2){0.f, 0.f};
    }
  } else asm volatile("s_nop 1" ::: "memory");
  GAP();
  (gapBf<X, PEEL, ZERO1, DK, DV, KR, SJ, Ms>(c, kslot_rd), ...);
  asm volatile("s_waitcnt lgkmcnt(0)" ::: "memory");
  GAP();
}
template <int X, bool PEEL, bool ZERO1, bool DK = true, bool DV = true, bool KR = true, int SJ = 0> DI void phaseBf(W4C& c, const int t) { phaseBf_<X, PEEL, ZERO1, DK, DV, KR, SJ>(c, t, make_iseq<32>{}); }
template <int X, int... Es> DI void f_exps(W4C& c, iseq<Es...>) { (f_exp<X, 1, Es>(c), ...); }
template <int X, int... Is> DI void f_fin(W4C& c, iseq<Is...>) { ((f_add<X, 1, Is>(c), f_pack<X, 1, Is>(c)), ...); }
template <int... Is> DI void k_reads(W4C& c, iseq<Is...>) { (k_read<Is>(c, 0u), ...); }

struct QPC { unsigned vtr, vwsrc, m0w, sofw, vx; rsrc_t srdW, srdX; bf16x4 wl[8], wu[8]; u32x4 xv[4][8]; };
template <int F> DI void qp_tr(QPC& q, unsigned vaddr) {
  constexpr int ks = F >> 2, db = F & 3;
  asm volatile("ds_read_b64_tr_b16 %0, %1 offset:%2" : "=v"(q.wl[F & 7]) : "v"(vaddr), "i"(4096 * ks + 512 * db) : "memory");
  asm volatile("ds_read_b64_tr_b16 %0, %1 offset:%2" : "=v"(q.wu[F & 7]) : "v"(vaddr), "i"(4096 * ks + 256 + 512 * db) : "memory");
}
template <int T, int Q> DI void qp_g(QPC& q) {
  if constexpr (Q < 4) dma16(q.m0w + T * 0x4000u + 1024u * Q, q.vwsrc, q.srdW, q.sofw + T * 64u * 8192u + 8192u * 8u * (Q >> 1) + 128u * (Q & 1));
  else {
    constexpr int J = Q - 4, a = 128 + 32 * (T & 3) + 4 * J;
    if constexpr (T >= 4 && T < 8) asm volatile("buffer_load_dwordx4 %0, %1, %2, %3 offen offset:%c4" : "=v"(q.xv[T & 3][J]) : "v"(q.vx), "s"(q.srdX), "s"((unsigned)((4 * T + (J & 3)) * 65536)), "i"((J >> 2) * 1024) : "memory");
    else asm volatile("buffer_load_dwordx4 a[%c3:%c4], %0, %1, %2 offen offset:%c5" :: "v"(q.vx), "s"(q.srdX), "s"((unsigned)((4 * T + (J & 3)) * 65536)), "i"(a), "i"(a + 3), "i"((J >> 2) * 1024) : "memory", ACLOB_HI);
  }
}
template <int T, int... Q> DI void qp_grp(QPC& q, iseq<Q...>) { (qp_g<T, Q>(q), ...); }
template <int TT, int M, int LG> DI void qp_mfma(QPC& q) {
  constexpr int f = M >> 1, ks = f >> 2, db = f & 3, qb = M & 1, ao = AO_(db, qb), ax = 128 + 32 * (TT & 3) + 16 * qb + 4 * ks;
  if constexpr (qb == 0) asm volatile("s_waitcnt lgkmcnt(%c2)" : "+v"(q.wl[f & 7]), "+v"(q.wu[f & 7]) : "i"(LG) : "memory");
  const bf16x8 wf = __builtin_shufflevector(q.wl[f & 7], q.wu[f & 7], 0, 1, 2, 3, 4, 5, 6, 7);
  if constexpr (TT == 0 && ks == 0) asm volatile("v_mfma_f32_32x32x16_bf16 a[%c1:%c2], %0, a[%c3:%c4], 0" :: "v"(wf), "i"(ao), "i"(ao + 15), "i"(ax), "i"(ax + 3) : ACLOB);
  else if constexpr (TT >= 4 && TT < 8) asm volatile("v_mfma_f32_32x32x16_bf16 a[%c2:%c3], %0, %1, a[%c2:%c3]" :: "v"(wf), "v"(q.xv[TT & 3][4 * qb + ks]), "i"(ao), "i"(ao + 15) : ACLOB);
  else asm volatile("v_mfma_f32_32x32x16_bf16 a[%c1:%c2], %0, a[%c3:%c4], a[%c1:%c2]" :: "v"(wf), "i"(ao), "i"(ao + 15), "i"(ax), "i"(ax + 3) : ACLOB);
}
template <int T, int J> DI void qp_gap(QPC& q, W4C& c, unsigned vrd) {
  if constexpr (J < 8) { if constexpr (T >= 0) qp_mfma<T, 24 + J, (T == 9 ? 0 : 6)>(q); }
  else qp_mfma<T + 1, J - 8, 6>(q);
  if constexpr (T < 9 && (J & 1) == 0) qp_tr<(J >> 1)>(q, vrd);
  if constexpr (T <= 1 && J < 24) qp_g<2 * T + 4 + J / 12, J % 12>(q);
  if constexpr ((T == 2 || T == 3) && (J & 1) && J < 24) qp_g<T + 6, (J >> 1)>(q);
  if constexpr (T == 4 && (J & 1) && J < 8)            dma16(c.m0k + 1024u * (J >> 1), c.vksrc[(J >> 1) & 1], c.srdK, c.sofk + KRS * 4u * (J >> 1));
  if constexpr (T == 4 && (J & 1) && J >= 8 && J < 16)  dma16(c.m0v + 1024u * ((J - 8) >> 1), c.vvsrc, c.srdV, c.sofv + KRS * 8u * ((J - 8) >> 2) + 128u * ((J >> 1) & 1));
  if constexpr (T == 4 && (J & 1) && J >= 16 && J < 24) dma16(c.m0k + 0x4000u + 1024u * ((J - 16) >> 1), c.vksrc[(J >> 1) & 1], c.srdK, KTILEB + c.sofk + KRS * 4u * ((J - 16) >> 1));
  if constexpr (T == 9 && J >= 6) { k_read<8 * (J - 6)>(c, 0u); k_read<8 * (J - 6) + 1>(c, 0u); k_read<8 * (J - 6) + 2>(c, 0u); k_read<8 * (J - 6) + 3>(c, 0u);
                                    k_read<8 * (J - 6) + 4>(c, 0u); k_read<8 * (J - 6) + 5>(c, 0u); k_read<8 * (J - 6) + 6>(c, 0u); k_read<8 * (J - 6) + 7>(c, 0u); }
  GAP();
}
template <int T, int VM, int... Js> DI void qp_step_(QPC& q, W4C& c, iseq<Js...>) {
  const unsigned vrd = q.vtr + (T + 1) * 0x4000u;
  constexpr int u = (T + 1) & 3;
  if constexpr (T + 1 >= 4 && T + 1 < 8) asm volatile("s_waitcnt vmcnt(%c8)\n\ts_barrier" : "+v"(q.xv[u][0]), "+v"(q.xv[u][1]), "+v"(q.xv[u][2]), "+v"(q.xv[u][3]), "+v"(q.xv[u][4]), "+v"(q.xv[u][5]), "+v"(q.xv[u][6]), "+v"(q.xv[u][7]) : "i"(VM) : "memory");
  else if constexpr (T < 9) asm volatile("s_waitcnt vmcnt(%c0)\n\ts_barrier" :: "i"(VM) : "memory");
  GAP();
  (qp_gap<T, Js>(q, c, vrd), ...);
}
template <int T, int VM> DI void qp_step(QPC& q, W4C& c) { qp_step_<T, VM>(q, c, make_iseq<(T == 9 ? 8 : 32)>{}); }
template <int R> DI u32x4 acc_pack8(float f, float* chk = nullptr) {
  float o[8];
  asm volatile("v_accvgpr_read_b32 %0, a[%c8]\n\tv_accvgpr_read_b32 %1, a[%c9]\n\tv_accvgpr_read_b32 %2, a[%c10]\n\tv_accvgpr_read_b32 %3, a[%c11]\n\t"
               "v_accvgpr_read_b32 %4, a[%c12]\n\tv_accvgpr_read_b32 %5, a[%c13]\n\tv_accvgpr_read_b32 %6, a[%c14]\n\tv_accvgpr_read_b32 %7, a[%c15]"
               : "=v"(o[0]), "=v"(o[1]), "=v"(o[2]), "=v"(o[3]), "=v"(o[4]), "=v"(o[5]), "=v"(o[6]), "=v"(o[7])
               : "i"(R), "i"(R + 1), "i"(R + 2), "i"(R + 3), "i"(R + 4), "i"(R + 5), "i"(R + 6), "i"(R + 7));
  u32x4 w;
#pragma unroll
  for (int e = 0; e < 4; ++e) w[e] = cvtpk(o[2 * e] * f, o[2 * e + 1] * f);
  if (chk) {
#pragma unroll
    for (int e = 0; e < 8; ++e) asm("v_fmac_f32_e32 %0, 0, %1" : "+v"(chk[e & 1]) : "v"(o[e]));
  }
  return w;
}
template <int I> DI void q_conv1(float C) {
  constexpr int qb = I >> 3, d0 = I & 7, aq = AQ_(qb, d0);
  const u32x4 w = acc_pack8<AO_(d0 >> 1, qb) + 8 * (d0 & 1)>(C);
  asm volatile("v_accvgpr_write_b32 a[%c4], %0\n\tv_accvgpr_write_b32 a[%c5], %1\n\tv_accvgpr_write_b32 a[%c6], %2\n\tv_accvgpr_write_b32 a[%c7], %3"
               :: "v"(w[0]), "v"(w[1]), "v"(w[2]), "v"(w[3]), "i"(aq), "i"(aq + 1), "i"(aq + 2), "i"(aq + 3) : ACLOB_HI);
}
template <int... Is> DI void q_conv(float C, iseq<Is...>) { (q_conv1<Is>(C), ...); }
template <int I> DI void o_store1(unsigned char* dst, float il0, float il1, float* chk) {
  constexpr int qb = I >> 3, j = I & 7;
  const u32x4 w = acc_pack8<AO_(j >> 1, qb) + 8 * (j & 1)>(qb ? il1 : il0, chk);
  *(u32x4*)(dst + qb * 1024u + (size_t)j * 65536u) = w;
}
template <int... Is> DI void o_store(unsigned char* dst, float il0, float il1, float* chk, iseq<Is...>) { (o_store1<Is>(dst, il0, il1, chk), ...); }
DI void gload16(u32x4& r, const void* p) { asm volatile("global_load_dwordx4 %0, %1, off" : "=v"(r) : "v"(p) : "memory"); }
DI void gstore16(void* p, const u32x4& r) { asm volatile("global_store_dwordx4 %0, %1, off\n\ts_nop 1" :: "v"(p), "v"(r) : "memory"); }

__global__ __launch_bounds__(256, 1)
void k_attn(const unsigned char* __restrict__ xnF, const unsigned short* __restrict__ wq, const unsigned short* __restrict__ Kc, const unsigned short* __restrict__ Vc,
            unsigned char* __restrict__ OF, const unsigned char* __restrict__ zF, unsigned char* __restrict__ z2nT,
            const unsigned short* __restrict__ w_sp, unsigned char* __restrict__ wspF,
            const unsigned short* __restrict__ wo, const unsigned short* __restrict__ w_out) {
  extern __shared__ __attribute__((aligned(16))) char lds[];
  const unsigned tid = threadIdx.x, lane = tid & 63u, wid = rfl(tid >> 6), l32 = lane & 31u, hi = lane >> 5;
  constexpr int T = NTILE;
  const float C = 0.12751743082f;
  const unsigned xcd = blockIdx.x & 7u, idx = blockIdx.x >> 3;
  const unsigned h = 8u * (xcd >> 1) + (idx & 7u), y = 4u * (xcd & 1u) + (idx >> 3);
  const unsigned rb0 = 8u * y + 2u * wid;
  W4C c; QPC q;
  TS(2, 0);
  c.ldsb = (unsigned)(size_t)lds;
#pragma unroll
  for (int j = 0; j < 4; ++j) c.vkb[j] = c.ldsb + KRING + 256u * l32 + 16u * ((2u * j + hi) ^ (l32 & 7u));
  c.vvb = c.ldsb + VRING + tr_lane<false, false>(lane, 0u);
#pragma unroll
  for (int par = 0; par < 2; ++par) c.vksrc[par] = KRS * (lane >> 4) + 16u * ((lane & 15u) ^ (4u * par + (lane >> 4)));
  c.vvsrc = KRS * ((lane >> 2) & 7u) + 64u * hi + 16u * (lane & 3u);
  c.m0k = rfl(c.ldsb + KRING + 4096u * wid); c.m0v = rfl(c.ldsb + VRING + 4096u * wid);
  c.sofk = KRS * 16u * wid; c.sofv = KRS * 16u * wid;
  c.srdK = mk_srd((const char*)Kc + (size_t)h * SEQ * 256u, SEQ * 256u);
  c.srdV = mk_srd((const char*)Vc + (size_t)h * SEQ * 256u, SEQ * 256u);
  q.vtr = c.ldsb + WRING + tr_lane<true, true>(lane, 2048u);
  q.vwsrc = 8192u * ((lane >> 2) & 7u) + 64u * hi + 16u * (lane & 3u);
  q.m0w = rfl(c.ldsb + WRING + 4096u * wid); q.sofw = 8192u * 16u * wid;
  q.vx = rb0 * 1024u + lane * 16u;
  q.srdW = mk_srd((const char*)wq + h * 256u, DM * 8192u - h * 256u);
  q.srdX = mk_srd(xnF, SEQ * DM * 2u);
  const unsigned srow = blockIdx.x * 8u + 2u * wid + hi;
  u32x4 zv[4];
#pragma unroll
  for (int i = 0; i < 4; ++i) { const unsigned pc = l32 + 32u * i, p = pc < 108u ? pc : 107u;
    gload16(zv[i], zF + ((size_t)((54u + (p >> 1)) * 64u + (srow >> 5)) * 64u + (p & 1u) * 32u + (srow & 31u)) * 16u); }
  asm volatile("s_nop 4" ::: "memory");
  qp_grp<0>(q, make_iseq<12>{}); qp_grp<1>(q, make_iseq<12>{});
  asm volatile("s_waitcnt vmcnt(24)" : "+v"(zv[0]), "+v"(zv[1]), "+v"(zv[2]), "+v"(zv[3]) :: "memory");
  {
    const bool v3 = l32 < 12u;
    float f[32], s = 0.f;
#pragma unroll
    for (int i = 0; i < 4; ++i)
#pragma unroll
      for (int e = 0; e < 4; ++e) { f[8 * i + 2 * e] = bflo(zv[i][e]); f[8 * i + 2 * e + 1] = bfhi(zv[i][e]); const float t2 = f[8 * i + 2 * e] + f[8 * i + 2 * e + 1]; s += (i < 3 || v3) ? t2 : 0.f; }
#pragma unroll
    for (int o = 16; o; o >>= 1) s += __shfl_xor(s, o);
    const float mean = s * (1.f / FH); float qq = 0.f;
#pragma unroll
    for (int i = 0; i < 32; ++i) { f[i] -= mean; qq += (i < 24 || v3) ? f[i] * f[i] : 0.f; }
#pragma unroll
    for (int o = 16; o; o >>= 1) qq += __shfl_xor(qq, o);
    const float rstd = 1.f / sqrtf(qq * (1.f / FH) + 1e-5f);
#pragma unroll
    for (int i = 0; i < 4; ++i) { const unsigned pc = l32 + 32u * i, p = pc < 108u ? pc : 107u;
      u32x4 w;
#pragma unroll
      for (int e = 0; e < 4; ++e) w[e] = cvtpk(f[8 * i + 2 * e] * rstd, f[8 * i + 2 * e + 1] * rstd);
      gstore16(z2nT + ((size_t)(blockIdx.x * 27u + (p >> 2)) * 8u + (srow & 7u)) * 64u + (p & 3u) * 16u, w); }
  }
  TS(2, 4); TS(2, 5);
  asm volatile("s_waitcnt lgkmcnt(0)" ::: "memory");
  qp_step<-1, 16>(q, c); qp_step<0, 28>(q, c); qp_step<1, 36>(q, c); qp_step<2, 48>(q, c); qp_step<3, 48>(q, c); qp_step<4, 48>(q, c);
  qp_step<5, 48>(q, c); qp_step<6, 36>(q, c); qp_step<7, 24>(q, c); qp_step<8, 8>(q, c); qp_step<9, 0>(q, c);
  asm volatile("s_nop 7\n\ts_nop 7" ::: "memory");
  q_conv(C, make_iseq<16>{});
  asm volatile("s_waitcnt lgkmcnt(0)\n\ts_nop 1" ::: "memory");
  GAP();
  TS(2, 1);
  { const unsigned srb = blockIdx.x >> 2, ks0 = (blockIdx.x & 3u) * 32u + wid * 8u;
    c.vsl = (srb * 32u + l32) * 4096u + ks0 * 32u + hi * 16u; c.vss = (ks0 * 64u + srb) * 1024u + lane * 16u;
    c.srdSL = mk_srd(w_sp, SEQ * SEQ * 2u); c.srdSS = mk_srd(wspF, SEQ * SEQ * 2u); c.ssl = 0u; c.sss = 0u; }
  phaseA<0, true>(c, 0);  phaseBf<0, true, false, true, true, true, 2>(c, 0);
  phaseAf<1, 1>(c, 1); phaseBf<1, false, true>(c, 1);
  { const unsigned gw = blockIdx.x * 4u + wid;
    const bool isw = gw < 640u; const unsigned go = isw ? gw : (gw < 775u ? gw - 640u : 134u);
    const rsrc_t srdP = mk_srd(isw ? wo : w_out, isw ? NH * DH * DM * 2u : FH * DM * 2u);
    asm volatile("s_mov_b32 m0, %0\n\ts_nop 4\n\tbuffer_load_dword %1, %2, %3 offen lds\n\tbuffer_load_dword %1, %2, %3 offen offset:64 lds"
                 :: "s"(c.ldsb + 0x10000u + wid * 1024u), "v"(lane * 128u), "s"(srdP), "s"(go * 8192u) : "m0", "memory"); }
  for (int t = 2; t < T - 2; t += 2) {
    const unsigned fi = (unsigned)(t >> 1), fl = fi < 7u ? fi : 7u, fs = fi - 1u < 7u ? fi - 1u : 7u;
    c.ssl = fl * 32u; c.sss = fs * 65536u;
    phaseAf<0>(c, t);        phaseBf<0, false, false, true, true, true, 3>(c, t);
    phaseAf<1, 2>(c, t + 1); phaseBf<1, false, false>(c, t + 1);
  }
  TS(2, 2);
  phaseAf<0>(c, T - 2); phaseBf<0, false, false, false, true, true, 1>(c, T - 2);
  phaseAf<1>(c, T - 1); phaseBf<1, false, false, false, false, false>(c, T - 1);
  unsigned lb; asm volatile("v_mbcnt_lo_u32_b32 %0, -1, 0\n\tv_mbcnt_hi_u32_b32 %0, -1, %0" : "=v"(lb));
  unsigned char* const odst = OF + ((size_t)(8u * h * 64u + rb0) * 64u + lb) * 16u;
  bool bad;
  {
    tr_reads(c, (unsigned)((T - 1) & 1) * 0x4000u, make_iseq<32>{});
    f_add<1, 0, 15>(c); f_pack<1, 0, 15>(c);
    f_exps<1>(c, make_iseq<32>{});
    f_fin<1>(c, make_iseq<16>{});
    asm volatile("s_waitcnt lgkmcnt(0)" : W4_VLO :: "memory"); asm volatile("" : W4_VUP :: "memory");
    GAP();
    v_concat(c);
    asm volatile("s_nop 1" ::: "memory");
    pv_all(c, make_iseq<32>{});
    float l[2];
#pragma unroll
    for (int qb = 0; qb < 2; ++qb) { const float rs = c.rs2[qb][0] + c.rs2[qb][1];
      auto pr = __builtin_amdgcn_permlane32_swap(__float_as_uint(rs), __float_as_uint(rs), false, false); l[qb] = __uint_as_float(pr[0]) + __uint_as_float(pr[1]); }
    asm volatile("s_nop 7\n\ts_nop 7" ::: "memory");
    float chk[2] = {0.f, 0.f};
    o_store(odst, __builtin_amdgcn_rcpf(l[0]), __builtin_amdgcn_rcpf(l[1]), chk, make_iseq<16>{});
    const bool ok = (l[0] < 0x1p64f) & (l[1] < 0x1p64f) & (chk[0] == 0.f) & (chk[1] == 0.f);
    volatile unsigned* flag = (volatile unsigned*)(lds + 0x12000);
    const unsigned wbad = __any((int)!ok) ? 1u : 0u;
    if (lb == 0u) flag[wid] = wbad;
    __syncthreads();
    bad = rfl(flag[0] | flag[1] | flag[2] | flag[3]) != 0u;
  }
  if (__builtin_expect(bad, 0)) {
    asm volatile("s_waitcnt vmcnt(0) lgkmcnt(0)\n\ts_nop 4" ::: "memory");
#pragma unroll
    for (int i = 0; i < 4; ++i) {
      dma16(c.m0k + 1024u * i, c.vksrc[i & 1], c.srdK, c.sofk + KRS * 4u * i);
      dma16(c.m0v + 1024u * i, c.vvsrc, c.srdV, c.sofv + KRS * 8u * (i >> 1) + 128u * (i & 1));
      dma16(c.m0k + 0x4000u + 1024u * i, c.vksrc[i & 1], c.srdK, KTILEB + c.sofk + KRS * 4u * i);
    }
    asm volatile("s_waitcnt vmcnt(0)\n\ts_barrier" ::: "memory");
    k_reads(c, make_iseq<16>{});
    asm volatile("s_waitcnt lgkmcnt(0)" ::: "memory");
    GAP();
    c.mhat[0] = c.mhat[1] = 0.f; c.lsum[0] = c.lsum[1] = 0.f; c.fct[0] = c.fct[1] = 1.f; c.pend = false;
    phaseA<0, true>(c, 0);  phaseB<0, true, false>(c, 0);
    phaseA<1, false>(c, 1); phaseB<1, false, true>(c, 1);
    for (int t = 2; t < T - 2; t += 2) {
      phaseA<0, false>(c, t);     phaseB<0, false, false>(c, t);
      phaseA<1, false>(c, t + 1); phaseB<1, false, false>(c, t + 1);
    }
    phaseA<0, false>(c, T - 2); phaseB<0, false, false, false, true, true>(c, T - 2);
    phaseA<1, false>(c, T - 1); phaseB<1, false, false, false, false, false>(c, T - 1);
    tr_reads(c, (unsigned)((T - 1) & 1) * 0x4000u, make_iseq<32>{});
#pragma unroll
    for (int e = 0; e < 32; ++e) c.S[1][1][e >> 4][e & 15] = __builtin_amdgcn_exp2f(c.S[1][1][e >> 4][e & 15]);
    packs<1>(c, make_iseq<32>{});
    rowsums<1>(c, iseq<8, 9, 10, 11, 12, 13, 14, 15, 24, 25, 26, 27, 28, 29, 30, 31>{});
    lsum_merge(c);
    asm volatile("s_waitcnt lgkmcnt(0)" : W4_VLO :: "memory"); asm volatile("" : W4_VUP :: "memory");
    GAP();
    v_concat(c);
    asm volatile("s_nop 1" ::: "memory");
    pv_all(c, make_iseq<32>{});
    asm volatile("s_nop 7\n\ts_nop 7" ::: "memory");
    unsigned lb2; asm volatile("v_mbcnt_lo_u32_b32 %0, -1, 0\n\tv_mbcnt_hi_u32_b32 %0, -1, %0" : "=v"(lb2));
    o_store(OF + ((size_t)(8u * h * 64u + rb0) * 64u + lb2) * 16u, __builtin_amdgcn_rcpf(c.lsum[0]), __builtin_amdgcn_rcpf(c.lsum[1]), nullptr, make_iseq<16>{});
  }
  TS(2, 3);
}

extern "C" void kernel_launch(void* const* d_in, const int* in_sizes, int n_in, void* d_out, int out_size, void* d_ws, size_t ws_size, hipStream_t stream) {
  const unsigned short* x = (const unsigned short*)d_in[0];
  const unsigned short* Kc = (const unsigned short*)d_in[1];
  const unsigned short* Vc = (const unsigned short*)d_in[2];
  const unsigned short* wq = (const unsigned short*)d_in[3];
  const unsigned short* wo = (const unsigned short*)d_in[4];
  const unsigned short* w_in = (const unsigned short*)d_in[6];
  const unsigned short* w_sp = (const unsigned short*)d_in[7];
  const unsigned short* b_sp = (const unsigned short*)d_in[8];
  const unsigned short* w_out = (const unsigned short*)d_in[9];
  unsigned char* ws = (unsigned char*)d_ws;
  (void)hipFuncSetAttribute((const void*)k_attn, hipFuncAttributeMaxDynamicSharedMemorySize, 163840);
  (void)hipFuncSetAttribute((const void*)k_zgemm, hipFuncAttributeMaxDynamicSharedMemorySize, 163840);
  (void)hipFuncSetAttribute((const void*)k_mid, hipFuncAttributeMaxDynamicSharedMemorySize, 163840);
  (void)hipFuncSetAttribute((const void*)k_out, hipFuncAttributeMaxDynamicSharedMemorySize, 110592);
  hipLaunchKernelGGL(k_ln, dim3(256), dim3(256), 0, stream, x, ws + WS_XN, w_in, wq);
  hipLaunchKernelGGL(k_zgemm, dim3(224), dim3(256), 163840, stream, ws + WS_XN, w_in, ws + WS_Z);
  hipLaunchKernelGGL(k_attn, dim3(256), dim3(256), 163840, stream, ws + WS_XN, wq, Kc, Vc, ws + WS_O, ws + WS_Z, ws + WS_Z2N, w_sp, ws + WS_WSP, wo, w_out);
  hipLaunchKernelGGL(k_mid, dim3(256), dim3(256), 163840, stream, ws + WS_O, wo, ws + WS_SP, ws + WS_WSP, ws + WS_Z2N, ws + WS_Z, b_sp, ws + WS_U);
  hipLaunchKernelGGL(k_out, dim3(160), dim3(256), 110592, stream, ws + WS_U, w_out, ws + WS_SP, x, (unsigned short*)d_out);
}
```

```cpp
#include <hip/hip_runtime.h>
#include <hip/hip_bf16.h>
#include <utility>

typedef float    f32x16 __attribute__((ext_vector_type(16)));
typedef float    f32x2  __attribute__((ext_vector_type(2)));
typedef float    f32x4  __attribute__((ext_vector_type(4)));
typedef __bf16   bf16x8 __attribute__((ext_vector_type(8)));
typedef __bf16   bf16x4 __attribute__((ext_vector_type(4)));
typedef __bf16   bf16x2 __attribute__((ext_vector_type(2)));
typedef unsigned u32x4  __attribute__((ext_vector_type(4)));
typedef unsigned u32x2  __attribute__((ext_vector_type(2)));
template <int... Is> using iseq = std::integer_sequence<int, Is...>;
template <int N> using make_iseq = std::make_integer_sequence<int, N>;
typedef __amdgpu_buffer_rsrc_t rsrc_t;

#define DI __device__ __forceinline__
#define GAP() __builtin_amdgcn_sched_barrier(0)
#define SRD_WORD3 0x00020000
#define A10(x) "a" #x "0", "a" #x "1", "a" #x "2", "a" #x "3", "a" #x "4", "a" #x "5", "a" #x "6", "a" #x "7", "a" #x "8", "a" #x "9"
#define ACLOB_LO "a0", "a1", "a2", "a3", "a4", "a5", "a6", "a7", "a8", "a9", A10(1), A10(2), A10(3), A10(4), A10(5), A10(6), A10(7), A10(8), A10(9), A10(10), A10(11), \
                 "a120", "a121", "a122", "a123", "a124", "a125", "a126", "a127"
#define ACLOB_HI "a128", "a129", A10(13), A10(14), A10(15), A10(16), A10(17), A10(18), A10(19), A10(20), A10(21), A10(22), A10(23), A10(24), \
                 "a250", "a251", "a252", "a253", "a254", "a255"
#define ACLOB ACLOB_LO, ACLOB_HI

constexpr int SEQ = 2048, DM = 640, NH = 32, DH = 128, FF = 1728, FH = 864;
constexpr size_t WS_XN  = 0;
constexpr size_t WS_Z   = WS_XN  + (size_t)SEQ * DM * 2;
constexpr size_t WS_O   = WS_Z   + (size_t)SEQ * FF * 2;
constexpr size_t WS_Z2N = WS_O   + (size_t)SEQ * NH * DH * 2;
constexpr size_t WS_WSP = WS_Z2N + (size_t)SEQ * FH * 2;
constexpr size_t WS_SP  = WS_WSP + (size_t)SEQ * SEQ * 2;
constexpr size_t WS_U   = WS_SP  + (size_t)2 * SEQ * DM * 4;
constexpr size_t WS_END = WS_U   + (size_t)SEQ * FH * 2;

DI unsigned rfl(unsigned x) { return __builtin_amdgcn_readfirstlane(x); }
DI unsigned cvtpk(float lo, float hi) { f32x2 v = {lo, hi}; bf16x2 b = __builtin_convertvector(v, bf16x2); return __builtin_bit_cast(unsigned, b); }
DI float bflo(unsigned w) { return __uint_as_float(w << 16); }
DI float bfhi(unsigned w) { return __uint_as_float(w & 0xffff0000u); }
DI rsrc_t mk_srd(const void* p, unsigned bytes) { return __builtin_amdgcn_make_buffer_rsrc((void*)p, (short)0, (int)bytes, SRD_WORD3); }
DI float max3f(float a, float b, float c) { float r; asm("v_max3_f32 %0, %1, %2, %3" : "=v"(r) : "v"(a), "v"(b), "v"(c)); return r; }
DI float max2f(float a, float b) { float r; asm("v_max_f32_e32 %0, %1, %2" : "=v"(r) : "v"(a), "v"(b)); return r; }
DI float fadd_s(float a, float b) { float r; asm("v_add_f32_e32 %0, %1, %2" : "=v"(r) : "v"(a), "v"(b)); return r; }
DI float fmul_s(float a, float b) { float r; asm("v_mul_f32_e32 %0, %1, %2" : "=v"(r) : "v"(a), "v"(b)); return r; }
DI float fsub_s(float a, float b) { float r; asm("v_sub_f32_e32 %0, %1, %2" : "=v"(r) : "v"(a), "v"(b)); return r; }
template <int N> DI void wait_vm_bar() { asm volatile("s_waitcnt vmcnt(%c0) lgkmcnt(0)\n\ts_barrier" :: "i"(N > 63 ? 63 : N) : "memory"); }
DI void dma16(unsigned m0v, unsigned voff, rsrc_t srd, unsigned soff) {
  asm volatile("s_mov_b32 m0, %0\n\ts_nop 0\n\tbuffer_load_dwordx4 %1, %2, %3 offen lds" :: "s"(m0v), "v"(voff), "s"(srd), "s"(soff) : "m0", "memory");
}
DI void touch8k(const char* p) { unsigned d0, d1; asm volatile("global_load_dword %0, %2, off\n\tglobal_load_dword %1, %2, off offset:64" : "=&v"(d0), "=&v"(d1) : "v"(p) : "memory"); }
template <bool NATK, bool SWAPN> DI unsigned tr_lane(unsigned lane, unsigned halfstride) {
  const unsigned c = lane & 3u, cs = SWAPN ? (((c & 1u) << 1) | (c >> 1)) : c, hi = lane >> 5;
  return (cs << 3) | (((lane >> 2) & 3u) << 6) | (((lane >> 4) & 1u) << 5) | (NATK ? hi * halfstride : (hi << 8));
}

#define TS(k, i)
__global__ __launch_bounds__(256) void k_ln(const unsigned short* __restrict__ x, unsigned char* __restrict__ xnF, const unsigned short* __restrict__ w_in, const unsigned short* __restrict__ wq) {
  const unsigned lane = threadIdx.x & 63u, wid = threadIdx.x >> 6, l = lane & 31u;
  if (blockIdx.x >= 256u) {
    const unsigned gw = (blockIdx.x - 256u) * 4u + wid;
    if (gw < 270u) touch8k((const char*)w_in + gw * 8192u + lane * 128u); else if (gw < 910u) touch8k((const char*)wq + (gw - 270u) * 8192u + lane * 128u);
    return;
  }
  const unsigned row = blockIdx.x * 8u + wid * 2u + (lane >> 5);
  const u32x4* src = (const u32x4*)(x + (size_t)row * DM);
  u32x4 v[3]; v[0] = src[l]; v[1] = src[l + 32]; v[2] = (l < 16u) ? src[l + 64] : (u32x4){0u, 0u, 0u, 0u};
  float f[24]; float s = 0.f;
#pragma unroll
  for (int i = 0; i < 3; ++i)
#pragma unroll
    for (int e = 0; e < 4; ++e) { f[8 * i + 2 * e] = bflo(v[i][e]); f[8 * i + 2 * e + 1] = bfhi(v[i][e]); s += f[8 * i + 2 * e] + f[8 * i + 2 * e + 1]; }
#pragma unroll
  for (int o = 16; o; o >>= 1) s += __shfl_xor(s, o);
  const float mean = s * (1.f / DM); float q = 0.f;
#pragma unroll
  for (int i = 0; i < 24; ++i) { f[i] -= mean; if (i < 16 || l < 16u) q += f[i] * f[i]; }
#pragma unroll
  for (int o = 16; o; o >>= 1) q += __shfl_xor(q, o);
  const float rstd = 1.f / sqrtf(q * (1.f / DM) + 1e-5f);
#pragma unroll
  for (int i = 0; i < 3; ++i) {
    const unsigned c = l + 32u * i;
    if (i < 2 || l < 16u) {
      u32x4 w;
#pragma unroll
      for (int e = 0; e < 4; ++e) w[e] = cvtpk(f[8 * i + 2 * e] * rstd, f[8 * i + 2 * e + 1] * rstd);
      *(u32x4*)(xnF + ((size_t)((c >> 1) * 64u + (row >> 5)) * 64u + (c & 1u) * 32u + (row & 31u)) * 16u) = w;
    }
  }
}

constexpr int LA = 6;
template <int NT, int KS> struct GemmC {
  static constexpr int P = NT * KS / 4, TILEB = NT * KS * 1024, G = P + KS, NM = NT * KS;
  unsigned vw[P];
  unsigned vx[KS];
  unsigned vtr;
  unsigned m0w;
  unsigned wstride;
  rsrc_t srdW, srdX;
  int kid;
  f32x16 acc[NT];
};
struct NoEx { static constexpr int N = 0; template <int J> DI void issue() {} };
template <int KS> constexpr int GX(int u, int ks) { return 32 + (u * KS + ks) * 4; }
template <int NT, int KS, bool NATK, int AS, int F> DI void g_tr(unsigned vaddr) {
  constexpr int ks = F / NT, nt = F % NT, a = 4 * AS;
  constexpr int o0 = NATK ? ((2 * ks) * NT + nt) * 512 : ((2 * ks) * NT + nt) * 512, o1 = NATK ? o0 + 256 : o0 + NT * 512;
  asm volatile("ds_read_b64_tr_b16 a[%c1:%c2], %0 offset:%c5\n\tds_read_b64_tr_b16 a[%c3:%c4], %0 offset:%c6" :: "v"(vaddr), "i"(a), "i"(a + 1), "i"(a + 2), "i"(a + 3), "i"(o0), "i"(o1) : "memory", ACLOB);
}
template <int NT, int KS, int XI, int KSI> DI void g_xload(GemmC<NT, KS>& c, unsigned soff) {
  constexpr int a = GX<KS>(XI, KSI);
  asm volatile("buffer_load_dwordx4 a[%c3:%c4], %0, %1, %2 offen" :: "v"(c.vx[KSI]), "s"(c.srdX), "s"(soff), "i"(a), "i"(a + 3) : "memory", ACLOB);
}
template <int NT, int KS, int AS, int XI, bool ZERO, int LG, int M> DI void g_mfma(GemmC<NT, KS>& c) {
  constexpr int ks = M / NT, nt = M % NT, a = 4 * AS, b = GX<KS>(XI, ks);
  if constexpr (ZERO && ks == 0) asm volatile("s_waitcnt lgkmcnt(%c5)\n\tv_mfma_f32_32x32x16_bf16 %0, a[%c1:%c2], a[%c3:%c4], 0" : "=v"(c.acc[nt]) : "i"(a), "i"(a + 3), "i"(b), "i"(b + 3), "i"(LG) : "memory", ACLOB);
  else asm volatile("s_waitcnt lgkmcnt(%c5)\n\tv_mfma_f32_32x32x16_bf16 %0, a[%c1:%c2], a[%c3:%c4], %0" : "+v"(c.acc[nt]) : "i"(a), "i"(a + 3), "i"(b), "i"(b + 3), "i"(LG) : "memory", ACLOB);
}
template <int NT, int KS, int D, int GS, int Q> DI void g_vmem(GemmC<NT, KS>& c, unsigned g) {
  constexpr int P = GemmC<NT, KS>::P, G = GemmC<NT, KS>::G, gi = Q / G, q = Q % G, slot = (GS + gi) % D;
  if constexpr (q < P) dma16(c.m0w + slot * GemmC<NT, KS>::TILEB + 1024u * q, c.vw[q], c.srdW, (g + gi) * c.wstride);
  else g_xload<NT, KS, slot, q - P>(c, (g + gi) * (KS * 65536u));
}
template <int NT, int KS, int D, int... Q> DI void g_pre(GemmC<NT, KS>& c, iseq<Q...>) { (g_vmem<NT, KS, D, 0, Q>(c, 0u), ...); }
template <int NT, int KS, bool NATK, int D, int TM, bool FIRST, bool LAST, int GS, int NG, bool EXS, class EX, int J>
DI void g_gap(GemmC<NT, KS>& c, EX& ex, unsigned vrd, unsigned g) {
  constexpr int NM = NT * KS, NOP = NG * GemmC<NT, KS>::G, OPG = (NOP + NM - 1) / NM, T1 = (TM + 1) % D;
  if constexpr (J < LA) { if constexpr (!FIRST) g_mfma<NT, KS, (TM * NM + NM - LA + J) & 7, TM, false, (LAST ? 2 * (LA - 1 - J) : 2 * (LA - 1)), NM - LA + J>(c); }
  else if constexpr (!LAST) g_mfma<NT, KS, (T1 * NM + J - LA) & 7, T1, FIRST, 2 * (LA - 1), J - LA>(c);
  if constexpr (!LAST) g_tr<NT, KS, NATK, (T1 * NM + J) & 7, J>(vrd);
  if constexpr (OPG * J < NOP) g_vmem<NT, KS, D, GS, OPG * J>(c, g);
  if constexpr (OPG == 2 && 2 * J + 1 < NOP) g_vmem<NT, KS, D, GS, 2 * J + 1>(c, g);
  if constexpr (EXS) { if constexpr (2 * J < EX::N) ex.template issue<2 * J>(); if constexpr (2 * J + 1 < EX::N) ex.template issue<2 * J + 1>(); }
  GAP();
}
template <int NT, int KS, bool NATK, int D, int TM, bool FIRST, bool LAST, int GS, int NG, bool EXS, int VM, class EX, int... Js>
DI void g_step_(GemmC<NT, KS>& c, EX& ex, unsigned g, iseq<Js...>) {
  const unsigned vrd = c.vtr + ((TM + 1) % D) * GemmC<NT, KS>::TILEB;
  if constexpr (!LAST) asm volatile("s_waitcnt vmcnt(%c0)\n\ts_barrier" :: "i"(VM > 63 ? 63 : VM) : "memory");
  if constexpr (FIRST) TS(c.kid, 1);
  GAP();
  (g_gap<NT, KS, NATK, D, TM, FIRST, LAST, GS, NG, EXS, EX, Js>(c, ex, vrd, g), ...);
}
constexpr int g_iss(int t, int NTILES, int PRE) {
  int i = 1;
  for (int u = -1; u <= t; ++u) { i += 2; if (i > u + 1 + PRE) i = u + 1 + PRE; if (i > NTILES - 1) i = NTILES - 1; }
  return i;
}
template <int NT, int KS, bool NATK, int NTILES, int D, int PRE, class EX, int T> DI void g_step_static(GemmC<NT, KS>& c, EX& ex) {
  constexpr int G = GemmC<NT, KS>::G, i0 = g_iss(T - 1, NTILES, PRE), i1 = g_iss(T, NTILES, PRE);
  constexpr bool done = (i0 == NTILES - 1), done_before = (g_iss(T - 2, NTILES, PRE) == NTILES - 1);
  constexpr int VM = (i0 - (T + 1)) * G + (done_before ? EX::N : 0);
  g_step_<NT, KS, NATK, D, (T + D) % D, (T == -1), (T == NTILES - 1), (i0 + 1) % D, i1 - i0, (done && !done_before), VM, EX>(c, ex, i0 + 1, make_iseq<(T == NTILES - 1) ? LA : NT * KS>{});
}
template <int NT, int KS, bool NATK, int NTILES, int D, int PRE, class EX, int T0, int... Ts> DI void g_tail(GemmC<NT, KS>& c, EX& ex, iseq<Ts...>) {
  (g_step_static<NT, KS, NATK, NTILES, D, PRE, EX, T0 + Ts>(c, ex), ...);
}
template <int NT, int KS, bool NATK, int D, int PRE, class EX, int... Ts> DI void g_body(GemmC<NT, KS>& c, EX& ex, int t, iseq<Ts...>) {
  (g_step_<NT, KS, NATK, D, Ts, false, false, (Ts + 1 + PRE) % D, 1, false, (PRE - 1) * GemmC<NT, KS>::G, EX>(c, ex, t + Ts + 1 + PRE, make_iseq<NT * KS>{}), ...);
}
template <int NT, int KS, bool NATK, int NTILES, int D, int PRE, class EX> DI void gemm_run(GemmC<NT, KS>& c, EX& ex) {
  static_assert(NTILES <= D || (PRE <= D - 2 && (D * NT * KS) % 8 == 0), "ring");
  static_assert(32 + D * KS * 4 <= 256 && 2 * NT * KS >= EX::N, "regs");
  TS(c.kid, 0);
  asm volatile("s_nop 4" ::: "memory");
  g_pre<NT, KS, D>(c, make_iseq<2 * GemmC<NT, KS>::G>{});
  if constexpr (NTILES <= D) g_tail<NT, KS, NATK, NTILES, D, PRE, EX, -1>(c, ex, make_iseq<NTILES + 1>{});
  else {
    constexpr int T0 = D * ((NTILES - 1 - PRE) / D);
    g_tail<NT, KS, NATK, NTILES, D, PRE, EX, -1>(c, ex, make_iseq<D + 1>{});
    for (int t = D; t < T0; t += D) g_body<NT, KS, NATK, D, PRE, EX>(c, ex, t, make_iseq<D>{});
    g_tail<NT, KS, NATK, NTILES, D, PRE, EX, T0>(c, ex, make_iseq<NTILES - T0>{});
  }
  asm volatile("s_waitcnt vmcnt(0)\n\ts_nop 7\n\ts_nop 7" ::: "memory");
#pragma unroll
  for (int nt = 0; nt < NT; ++nt) asm volatile("" : "+v"(c.acc[nt]));
  TS(c.kid, 2);
  GAP();
}
template <int NT, int KS> DI void set_vx(GemmC<NT, KS>& c, unsigned v) {
#pragma unroll
  for (int k = 0; k < KS; ++k) c.vx[k] = v + k * 65536u;
}
DI void bload16(u32x4& r, unsigned voff, rsrc_t srd, unsigned soff) { asm volatile("buffer_load_dwordx4 %0, %1, %2, %3 offen" : "=v"(r) : "v"(voff), "s"(srd), "s"(soff) : "memory"); }
template <int NT, int KS, bool NATK, bool SWAPN> DI void gemm_setup_rm(GemmC<NT, KS>& c, unsigned ldsb, unsigned lane, unsigned wid, unsigned ldB) {
  constexpr int P = GemmC<NT, KS>::P;
#pragma unroll
  for (int q = 0; q < P; ++q) { const unsigned p = wid * P + q, kg = p / (NT / 2), cp = p % (NT / 2);
    c.vw[q] = ldB * (8u * kg + ((lane >> 2) & 7u)) + 128u * cp + 64u * (lane >> 5) + 16u * (lane & 3u); }
  c.wstride = ldB * 16u * KS;
  c.m0w = rfl(ldsb + 1024u * P * wid);
  c.vtr = ldsb + tr_lane<NATK, SWAPN>(lane, NT * 512u);
}

DI float gelu_tanh(float v) {
  const float y2 = v * (-2.3022081985f - 0.1029432397f * v * v);
  return v * __builtin_amdgcn_rcpf(1.f + __builtin_amdgcn_exp2f(y2));
}
__global__ __launch_bounds__(256, 1) void k_zgemm(const unsigned char* __restrict__ xnF, const unsigned short* __restrict__ w_in, unsigned char* __restrict__ zF) {
  extern __shared__ __attribute__((aligned(16))) char lds[];
  const unsigned lane = threadIdx.x & 63u, wid = rfl(threadIdx.x >> 6);
  const unsigned xcd = blockIdx.x & 7u, idx = blockIdx.x >> 3;
  const unsigned rb128 = 4u * (xcd >> 1) + idx / 7u, cb = 7u * (xcd & 1u) + idx % 7u, rb = 4u * rb128 + wid;
  GemmC<4, 5> c; c.kid = 1;
  gemm_setup_rm<4, 5, true, false>(c, (unsigned)(size_t)lds, lane, wid, FF * 2u);
  c.srdW = mk_srd((const char*)w_in + cb * 256u, DM * FF * 2u - cb * 256u);
  c.srdX = mk_srd(xnF, SEQ * DM * 2u);
  set_vx(c, rb * 1024u + lane * 16u);
  NoEx ex; gemm_run<4, 5, true, 8, 8, 6>(c, ex);
#pragma unroll
  for (int nt = 0; nt < 4; ++nt) {
    const unsigned ct = 4u * cb + nt;
    if (ct < 54u) {
#pragma unroll
      for (int a = 0; a < 2; ++a) {
        u32x4 w;
#pragma unroll
        for (int e = 0; e < 4; ++e) w[e] = cvtpk(gelu_tanh(c.acc[nt][8 * a + 2 * e]), gelu_tanh(c.acc[nt][8 * a + 2 * e + 1]));
        *(u32x4*)(zF + ((size_t)((2u * ct + a) * 64u + rb) * 64u + lane) * 16u) = w;
      }
    }
  }
}

template <int NT> struct ExZ { static constexpr int N = 2 * NT; u32x4 r[N]; unsigned voff; rsrc_t srd; template <int J> DI void issue() { bload16(r[J], voff, srd, J * 65536u); } };
template <int NT> DI void spatial_unit(char* lds, unsigned lane, unsigned wid, unsigned rb, unsigned ct0, const unsigned char* wspF, const unsigned char* z2nT,
                                       const unsigned char* zF, const unsigned short* b_sp, unsigned char* uF) {
  GemmC<NT, 4> c; c.kid = 3;
  const unsigned ldsb = (unsigned)(size_t)lds;
#pragma unroll
  for (int q = 0; q < NT; ++q) { const unsigned sg = 2u * (wid * NT + q) + (lane >> 5), kg = sg / NT, nt = sg % NT;
    c.vw[q] = (kg * 27u + ct0 + nt) * 512u + (lane & 31u) * 16u; }
  c.wstride = 8u * 27u * 512u;
  c.m0w = rfl(ldsb + 1024u * NT * wid);
  c.vtr = ldsb + tr_lane<true, true>(lane, NT * 512u);
  c.srdW = mk_srd(z2nT, SEQ * FH * 2u);
  c.srdX = mk_srd(wspF, SEQ * SEQ * 2u);
  set_vx(c, rb * 1024u + lane * 16u);
  const float bias = bflo((unsigned)b_sp[rb * 32u + (lane & 31u)]);
  ExZ<NT> ex; ex.voff = (2u * ct0 * 64u + rb) * 1024u + lane * 16u; ex.srd = mk_srd(zF, SEQ * FF * 2u);
  asm volatile("s_waitcnt vmcnt(0)" :: "v"(bias) : "memory");
  gemm_run<NT, 4, true, 32, 8, 6>(c, ex);
#pragma unroll
  for (int j = 0; j < 2 * NT; ++j) asm volatile("" : "+v"(ex.r[j]));
#pragma unroll
  for (int nt = 0; nt < NT; ++nt)
#pragma unroll
    for (int a = 0; a < 2; ++a) {
      const unsigned ks = 2u * (ct0 + nt) + a;
      const u32x4 z1 = ex.r[2 * nt + a];
      u32x4 w;
#pragma unroll
      for (int e = 0; e < 4; ++e) w[e] = cvtpk(bflo(z1[e]) * (c.acc[nt][8 * a + 2 * e] + bias), bfhi(z1[e]) * (c.acc[nt][8 * a + 2 * e + 1] + bias));
      *(u32x4*)(uF + ((size_t)(ks * 64u + rb) * 64u + lane) * 16u) = w;
    }
}
__global__ __launch_bounds__(256, 1) void k_mid(const unsigned char* __restrict__ OF, const unsigned short* __restrict__ wo, unsigned char* __restrict__ sP,
                                                const unsigned char* __restrict__ wspF, const unsigned char* __restrict__ z2nT, const unsigned char* __restrict__ zF,
                                                const unsigned short* __restrict__ b_sp, unsigned char* __restrict__ uF) {
  extern __shared__ __attribute__((aligned(16))) char lds[];
  const unsigned lane = threadIdx.x & 63u, wid = rfl(threadIdx.x >> 6);
  const unsigned xcd = blockIdx.x & 7u, idx = blockIdx.x >> 3;
  if (idx >= 12u) {
    const unsigned i = idx - 12u, kh = xcd & 1u, rb128 = 4u * (xcd >> 1) + i / 5u, cb = i % 5u, rb = 4u * rb128 + wid;
    GemmC<4, 4> c; c.kid = 3;
    gemm_setup_rm<4, 4, false, true>(c, (unsigned)(size_t)lds, lane, wid, DM * 2u);
    const unsigned wofs = kh * 2048u * DM * 2u + cb * 256u;
    c.srdW = mk_srd((const char*)wo + wofs, NH * DH * DM * 2u - wofs);
    c.srdX = mk_srd(OF, SEQ * NH * DH * 2u);
    set_vx(c, (kh * 128u * 64u + rb) * 1024u + lane * 16u);
    NoEx ex; gemm_run<4, 4, false, 32, 8, 6>(c, ex);
#pragma unroll
    for (int nt = 0; nt < 4; ++nt)
#pragma unroll
      for (int a = 0; a < 2; ++a) {
        u32x4 w;
#pragma unroll
        for (int e = 0; e < 4; ++e) w[e] = cvtpk(c.acc[nt][8 * a + 2 * e], c.acc[nt][8 * a + 2 * e + 1]);
        *(u32x4*)(sP + ((size_t)(((kh * 64u + rb) * 20u + 4u * cb + nt) * 2u + a) * 64u + lane) * 16u) = w;
      }
  } else {
    const unsigned rb128 = 4u * (xcd >> 1) + idx / 3u, j = 3u * (xcd & 1u) + idx % 3u, rb = 4u * rb128 + wid;
    if (j & 1u) spatial_unit<4>(lds, lane, wid, rb, 15u + 4u * (j >> 1), wspF, z2nT, zF, b_sp, uF);
    else        spatial_unit<5>(lds, lane, wid, rb, 5u * (j >> 1), wspF, z2nT, zF, b_sp, uF);
  }
}

struct ExO { static constexpr int N = 12; u32x4 r[N]; unsigned vs, vxx; rsrc_t srdS, srdXx;
  template <int J> DI void issue() { if constexpr (J < 8) bload16(r[J], vs, srdS, (J >> 2) * (64u * 20u * 2048u) + (J & 3) * 1024u); else bload16(r[J], vxx, srdXx, (J - 8) * 32u); } };
__global__ __launch_bounds__(256, 1) void k_out(const unsigned char* __restrict__ uF, const unsigned short* __restrict__ w_out, const unsigned char* __restrict__ sP,
                                                const unsigned short* __restrict__ x, unsigned short* __restrict__ out) {
  extern __shared__ __attribute__((aligned(16))) char lds[];
  const unsigned lane = threadIdx.x & 63u, wid = rfl(threadIdx.x >> 6);
  const unsigned xcd = blockIdx.x & 7u, idx = blockIdx.x >> 3;
  const unsigned rb128 = 4u * (xcd >> 1) + idx / 5u, cb = 5u * (xcd & 1u) + idx % 5u, rb = 4u * rb128 + wid;
  GemmC<2, 6> c; c.kid = 4;
  gemm_setup_rm<2, 6, false, true>(c, (unsigned)(size_t)lds, lane, wid, DM * 2u);
  c.srdW = mk_srd((const char*)w_out + cb * 128u, FH * DM * 2u - cb * 128u);
  c.srdX = mk_srd(uF, SEQ * FH * 2u);
  set_vx(c, rb * 1024u + lane * 16u);
  const unsigned row = rb * 32u + (lane & 31u), hi = lane >> 5;
  ExO ex; ex.vs = (rb * 20u + 2u * cb) * 2048u + lane * 16u; ex.vxx = row * 1280u + cb * 128u + hi * 16u;
  ex.srdS = mk_srd(sP, 2u * SEQ * DM * 2u); ex.srdXx = mk_srd(x, SEQ * DM * 2u);
  gemm_run<2, 6, false, 9, 9, 6>(c, ex);
#pragma unroll
  for (int j = 0; j < 12; ++j) asm volatile("" : "+v"(ex.r[j]));
#pragma unroll
  for (int nt = 0; nt < 2; ++nt) {
    const unsigned ct = 2u * cb + nt;
#pragma unroll
    for (int a = 0; a < 2; ++a) {
      const size_t eo = (size_t)row * DM + 32u * ct + 16u * a + 8u * hi;
      const u32x4 xv = ex.r[8 + 2 * nt + a];
      float sv[8];
#pragma unroll
      for (int e = 0; e < 4; ++e) { const unsigned p0 = ex.r[2 * nt + a][e], p1 = ex.r[4 + 2 * nt + a][e]; sv[2 * e] = bflo(p0) + bflo(p1); sv[2 * e + 1] = bfhi(p0) + bfhi(p1); }
      u32x4 w;
#pragma unroll
      for (int e = 0; e < 4; ++e) {
        const float b0 = c.acc[nt][8 * a + 2 * e], b1 = c.acc[nt][8 * a + 2 * e + 1];
        const float g0 = b0 * __builtin_amdgcn_rcpf(1.f + __builtin_amdgcn_exp2f(-1.4426950408889634f * b0));
        const float g1 = b1 * __builtin_amdgcn_rcpf(1.f + __builtin_amdgcn_exp2f(-1.4426950408889634f * b1));
        w[e] = cvtpk(bflo(xv[e]) + sv[2 * e] * g0, bfhi(xv[e]) + sv[2 * e + 1] * g1);
      }
      *(u32x4*)(out + eo) = w;
    }
  }
}

constexpr int NTILE = SEQ / 64;
constexpr unsigned KRS = 256u, KTILEB = 64u * KRS;
constexpr float THR = 8.0f;
#define KRING 0x0000u
#define VRING 0x10000u
#define WRING 0x0u
DI float w4_aone() { unsigned l; asm volatile("v_mbcnt_lo_u32_b32 %0, -1, 0\n\tv_mbcnt_hi_u32_b32 %0, -1, %0" : "=v"(l)); return l < 32u ? 1.f : 0.f; }
DI void negm_bcast(f32x16& negm, float aone, float neg_mhat) { asm volatile("s_nop 1\n\tv_mfma_f32_32x32x2_f32 %0, %1, %2, 0" : "+v"(negm) : "v"(aone), "v"(neg_mhat)); }
struct W4C {
  unsigned vkb[4], vvb, vksrc[2], vvsrc, m0k, m0v, sofk, sofv, ldsb;
  rsrc_t srdK, srdV;
  f32x16 S[2][2][2];
  f32x16 NEGM[2];
  bf16x8 Vf[4][4];
  bf16x8 Pk[2][4];
  bf16x4 vlo[4][4], vup[4][4];
  float mhat[2], lsum[2], fct[2]; bool pend;
  float rs0[2], rs1[2], pmx0[2], pmx1[2], rmax[2], delta[2];
  unsigned pm0[8], psf[8];
  u32x4 sw[2]; unsigned vsl, vss, ssl, sss; rsrc_t srdSL, srdSS;
};
constexpr int AO_(int db, int qb) { return 16 * (2 * db + qb); }
constexpr int AQ_(int qb, int d0) { return 128 + 32 * qb + 4 * d0; }
constexpr int AK_(int kb, int d0) { return 192 + 32 * kb + 4 * d0; }
template <int I, int SO = 0> DI void tr_read(W4C& c, unsigned vslot) {
  constexpr int ks = I >> 3, db = (I >> 1) & 3;
  if constexpr ((I & 1) == 0) asm volatile("ds_read_b64_tr_b16 %0, %1 offset:%2" : "=v"(c.vlo[db][ks]) : "v"(c.vvb + vslot), "i"(SO + 4096 * ks + 512 * db) : "memory");
  else                        asm volatile("ds_read_b64_tr_b16 %0, %1 offset:%2" : "=v"(c.vup[db][ks]) : "v"(c.vvb + vslot), "i"(SO + 4096 * ks + 2048 + 512 * db) : "memory");
}
template <int I, int SO = 0> DI void k_read(W4C& c, unsigned slot) {
  constexpr int kb = I >> 3, d0 = I & 7;
  asm volatile("ds_read_b128 a[%c1:%c2], %0 offset:%c3" :: "v"(c.vkb[d0 & 3] + slot), "i"(AK_(kb, d0)), "i"(AK_(kb, d0) + 3), "i"(SO + 8192 * kb + 128 * (d0 >> 2)) : "memory", ACLOB_HI);
}
template <int X, bool FIRST, int M> DI void qk_mfma(W4C& c) {
  constexpr int d0 = M >> 2, kb = (M >> 1) & 1, qb = M & 1, ak = AK_(kb, d0), aq = AQ_(qb, d0);
  if constexpr (d0 == 0) {
    if constexpr (FIRST) asm volatile("v_mfma_f32_32x32x16_bf16 %0, a[%c1:%c2], a[%c3:%c4], 0" : "=&v"(c.S[X][kb][qb]) : "i"(ak), "i"(ak + 3), "i"(aq), "i"(aq + 3) : ACLOB);
    else                 asm volatile("v_mfma_f32_32x32x16_bf16 %0, a[%c2:%c3], a[%c4:%c5], %1" : "=&v"(c.S[X][kb][qb]) : "v"(c.NEGM[qb]), "i"(ak), "i"(ak + 3), "i"(aq), "i"(aq + 3) : ACLOB);
  } else asm volatile("v_mfma_f32_32x32x16_bf16 %0, a[%c1:%c2], a[%c3:%c4], %0" : "+v"(c.S[X][kb][qb]) : "i"(ak), "i"(ak + 3), "i"(aq), "i"(aq + 3) : ACLOB);
}
template <int M, bool ZERO> DI void pv_mfma(W4C& c) {
  constexpr int ks = M >> 3, db = (M >> 1) & 3, qb = M & 1, ao = AO_(db, qb);
  if constexpr (ZERO && ks == 0) asm volatile("v_mfma_f32_32x32x16_bf16 a[%c2:%c3], %0, %1, 0" :: "v"(c.Vf[db][ks]), "v"(c.Pk[qb][ks]), "i"(ao), "i"(ao + 15) : ACLOB_LO);
  else                         asm volatile("v_mfma_f32_32x32x16_bf16 a[%c2:%c3], %0, %1, a[%c2:%c3]" :: "v"(c.Vf[db][ks]), "v"(c.Pk[qb][ks]), "i"(ao), "i"(ao + 15) : ACLOB_LO);
}
template <int X, int QB, int KS, int P> DI void pack1(W4C& c) {
  u32x4 w = __builtin_bit_cast(u32x4, c.Pk[QB][KS]);
  w[P] = cvtpk(c.S[X][KS >> 1][QB][8 * (KS & 1) + 2 * P], c.S[X][KS >> 1][QB][8 * (KS & 1) + 2 * P + 1]);
  c.Pk[QB][KS] = __builtin_bit_cast(bf16x8, w);
}
template <int X, int J> DI void rowsum1(W4C& c) {
  constexpr int qb = J >> 4, ks = (J >> 2) & 3, p = J & 3;
  c.rs0[qb] = fadd_s(c.rs0[qb], c.S[X][ks >> 1][qb][8 * (ks & 1) + 2 * p]); c.rs1[qb] = fadd_s(c.rs1[qb], c.S[X][ks >> 1][qb][8 * (ks & 1) + 2 * p + 1]);
}
template <int X, bool FIRST, int M> DI void gapA(W4C& c, unsigned vslot, const int t) {
  constexpr int Xp = X ^ 1;
  qk_mfma<X, FIRST, M>(c);
  if constexpr (M >= 16 && M <= 31) {
    constexpr int q = (M - 16) >> 1;
    if constexpr (((M - 16) & 1) == 0) {
      if constexpr (q < 4) c.pm0[q] = c.m0k + (unsigned)((t + 2) & 1) * 0x4000u + 1024u * q; else c.pm0[q] = c.m0v + (unsigned)((t + 1) & 1) * 0x4000u + 1024u * (q - 4);
      asm volatile("" : "+s"(c.pm0[q]));
    } else {
      if constexpr (q < 4) c.psf[q] = KTILEB * (unsigned)(t + 2) + c.sofk + KRS * 4u * q; else c.psf[q] = KTILEB * (unsigned)(t + 1) + c.sofv + KRS * 8u * ((q - 4) >> 1) + 128u * ((q - 4) & 1);
      asm volatile("" : "+s"(c.psf[q]));
    }
  }
  if constexpr (M == 31) asm volatile("s_nop 4" : "+s"(c.pm0[0]), "+s"(c.pm0[1]), "+s"(c.pm0[2]), "+s"(c.pm0[3]), "+s"(c.pm0[4]), "+s"(c.pm0[5]), "+s"(c.pm0[6]), "+s"(c.pm0[7]),
                                                  "+s"(c.psf[0]), "+s"(c.psf[1]), "+s"(c.psf[2]), "+s"(c.psf[3]), "+s"(c.psf[4]), "+s"(c.psf[5]), "+s"(c.psf[6]), "+s"(c.psf[7]));
  if constexpr (!FIRST && M == 8) asm volatile("" :: "v"(c.NEGM[0]), "v"(c.NEGM[1]));
  if constexpr (!FIRST) {
    constexpr int base = M + (M >= 4) + (M >= 8) + (M >= 12) + (M >= 16) + (M >= 20) + (M >= 24);
    if constexpr (M <= 25) tr_read<base>(c, vslot);
    if constexpr (M == 3 || M == 7 || M == 11 || M == 15 || M == 19 || M == 23) tr_read<base + 1>(c, vslot);
    if constexpr (M < 16) {
      constexpr int e0 = 2 * M, e1 = 2 * M + 1;
      c.S[Xp][1][e0 >> 4][e0 & 15] = __builtin_amdgcn_exp2f(c.S[Xp][1][e0 >> 4][e0 & 15]);
      c.S[Xp][1][e1 >> 4][e1 & 15] = __builtin_amdgcn_exp2f(c.S[Xp][1][e1 >> 4][e1 & 15]);
      pack1<Xp, (M >> 3), ((M >> 2) & 1), (M & 3)>(c);
    } else {
      constexpr int cc = M - 16;
      pack1<Xp, (cc >> 3), 2 + ((cc >> 2) & 1), (cc & 3)>(c);
      rowsum1<Xp, 16 * (cc >> 3) + 8 + 4 * ((cc >> 2) & 1) + (cc & 3)>(c);
    }
  }
  GAP();
}
#define W4_VLO "+v"(c.vlo[0][0]), "+v"(c.vlo[0][1]), "+v"(c.vlo[0][2]), "+v"(c.vlo[0][3]), "+v"(c.vlo[1][0]), "+v"(c.vlo[1][1]), "+v"(c.vlo[1][2]), "+v"(c.vlo[1][3]), \
               "+v"(c.vlo[2][0]), "+v"(c.vlo[2][1]), "+v"(c.vlo[2][2]), "+v"(c.vlo[2][3]), "+v"(c.vlo[3][0]), "+v"(c.vlo[3][1]), "+v"(c.vlo[3][2]), "+v"(c.vlo[3][3])
#define W4_VUP "+v"(c.vup[0][0]), "+v"(c.vup[0][1]), "+v"(c.vup[0][2]), "+v"(c.vup[0][3]), "+v"(c.vup[1][0]), "+v"(c.vup[1][1]), "+v"(c.vup[1][2]), "+v"(c.vup[1][3]), \
               "+v"(c.vup[2][0]), "+v"(c.vup[2][1]), "+v"(c.vup[2][2]), "+v"(c.vup[2][3]), "+v"(c.vup[3][0]), "+v"(c.vup[3][1]), "+v"(c.vup[3][2]), "+v"(c.vup[3][3])
DI void v_concat(W4C& c) {
#pragma unroll
  for (int ks = 0; ks < 4; ++ks)
#pragma unroll
    for (int db = 0; db < 4; ++db) c.Vf[db][ks] = __builtin_shufflevector(c.vlo[db][ks], c.vup[db][ks], 0, 1, 2, 3, 4, 5, 6, 7);
}
DI void lsum_merge(W4C& c) {
#pragma unroll
  for (int qb = 0; qb < 2; ++qb) {
    const float rs = fadd_s(c.rs0[qb], c.rs1[qb]);
    auto pr = __builtin_amdgcn_permlane32_swap(__float_as_uint(rs), __float_as_uint(rs), false, false);
    c.lsum[qb] = fadd_s(c.lsum[qb], fadd_s(__uint_as_float(pr[0]), __uint_as_float(pr[1])));
  }
}
template <int X, bool FIRST, int VMA, int... Ms> DI void phaseA_(W4C& c, const int t, iseq<Ms...>) {
  const unsigned vslot = (unsigned)((t - 1) & 1) * 0x4000u;
  (gapA<X, FIRST, Ms>(c, vslot, t), ...);
  if constexpr (!FIRST) lsum_merge(c);
  if constexpr (FIRST) asm volatile("s_waitcnt vmcnt(0) lgkmcnt(0)\n\ts_barrier" ::: "memory");
  else { asm volatile("s_waitcnt vmcnt(%c16) lgkmcnt(0)" : W4_VLO : "i"(VMA) : "memory"); asm volatile("s_barrier" : W4_VUP :: "memory"); }
  GAP();
  if constexpr (!FIRST) v_concat(c);
}
template <int X, bool FIRST, int VMA = 0> DI void phaseA(W4C& c, const int t) { phaseA_<X, FIRST, VMA>(c, t, make_iseq<32>{}); }

template <int X, bool PEEL, bool ZERO1, bool DK, bool DV, bool KR, int SJ, int M> DI void gapB(W4C& c, const unsigned kslot_rd) {
  if constexpr (!PEEL) pv_mfma<M, ZERO1>(c);
  if constexpr (M < 4 && DK) dma16(c.pm0[M], c.vksrc[M & 1], c.srdK, c.psf[M]);
  else if constexpr (M >= 4 && M < 8 && DV) dma16(c.pm0[M], c.vvsrc, c.srdV, c.psf[M]);
  if constexpr (M >= 1 && M <= 16 && KR) k_read<M - 1>(c, kslot_rd);
  if constexpr (M >= 8 && M < 16) {
    constexpr int i = M - 8, q = i >> 2, r = 4 * (i & 3);
    if constexpr ((i & 3) == 0) { c.pmx0[q] = max3f(c.S[X][0][q][0], c.S[X][0][q][1], c.S[X][1][q][0]); c.pmx1[q] = max3f(c.S[X][0][q][2], c.S[X][0][q][3], c.S[X][1][q][1]);
                                  c.pmx0[q] = max3f(c.pmx0[q], c.S[X][1][q][2], c.S[X][1][q][3]); }
    else                        { c.pmx0[q] = max3f(c.pmx0[q], c.S[X][0][q][r], c.S[X][0][q][r + 1]); c.pmx1[q] = max3f(c.pmx1[q], c.S[X][0][q][r + 2], c.S[X][0][q][r + 3]);
                                  c.pmx0[q] = max3f(c.pmx0[q], c.S[X][1][q][r], c.S[X][1][q][r + 1]); c.pmx1[q] = max3f(c.pmx1[q], c.S[X][1][q][r + 2], c.S[X][1][q][r + 3]); }
  }
  if constexpr (M == 16 || M == 17) {
    constexpr int q = M - 16; const float mm = max2f(c.pmx0[q], c.pmx1[q]);
    auto pr = __builtin_amdgcn_permlane32_swap(__float_as_uint(mm), __float_as_uint(mm), false, false);
    c.rmax[q] = max2f(__uint_as_float(pr[0]), __uint_as_float(pr[1]));
  }
  if constexpr (M == 18) {
    if constexpr (PEEL) {
#pragma unroll
      for (int q = 0; q < 2; ++q) { c.mhat[q] = c.rmax[q]; c.lsum[q] = 0.f; c.fct[q] = 1.f;
#pragma unroll
        for (int kb = 0; kb < 2; ++kb) { _Pragma("unroll") for (int e = 0; e < 16; ++e) c.S[X][kb][q][e] = fsub_s(c.S[X][kb][q][e], c.rmax[q]); }
        negm_bcast(c.NEGM[q], w4_aone(), -c.mhat[q]);
      }
    } else {
      const bool grew = (c.rmax[0] > THR) | (c.rmax[1] > THR);
      if (__builtin_expect(__any((int)grew), 0)) {
#pragma unroll
        for (int q = 0; q < 2; ++q) { const float dl = max2f(c.rmax[q], 0.f); c.delta[q] = dl; c.mhat[q] = fadd_s(c.mhat[q], dl); c.fct[q] = __builtin_amdgcn_exp2f(-dl);
#pragma unroll
          for (int kb = 0; kb < 2; ++kb) { _Pragma("unroll") for (int e = 0; e < 16; ++e) c.S[X][kb][q][e] = fsub_s(c.S[X][kb][q][e], dl); }
          negm_bcast(c.NEGM[q], w4_aone(), -c.mhat[q]);
        }
        c.pend = true;
      }
    }
  }
  if constexpr (M == 18) { c.rs0[0] = c.rs0[1] = c.rs1[0] = c.rs1[1] = 0.f; }
  if constexpr (M >= 19) {
    constexpr int g = M - 19, e_lo = (32 * g) / 13, e_hi = (32 * (g + 1)) / 13;
    {
      constexpr int p_lo = g ? (32 * (g - 1)) / 13 : 0, p_hi = g ? (32 * g) / 13 : 0;
      if constexpr (p_lo + 0 < p_hi) { constexpr int e = p_lo + 0; if constexpr (e & 1) c.rs1[e >> 4] = fadd_s(c.rs1[e >> 4], c.S[X][0][e >> 4][e & 15]); else c.rs0[e >> 4] = fadd_s(c.rs0[e >> 4], c.S[X][0][e >> 4][e & 15]); }
      if constexpr (p_lo + 1 < p_hi) { constexpr int e = p_lo + 1; if constexpr (e & 1) c.rs1[e >> 4] = fadd_s(c.rs1[e >> 4], c.S[X][0][e >> 4][e & 15]); else c.rs0[e >> 4] = fadd_s(c.rs0[e >> 4], c.S[X][0][e >> 4][e & 15]); }
      if constexpr (p_lo + 2 < p_hi) { constexpr int e = p_lo + 2; if constexpr (e & 1) c.rs1[e >> 4] = fadd_s(c.rs1[e >> 4], c.S[X][0][e >> 4][e & 15]); else c.rs0[e >> 4] = fadd_s(c.rs0[e >> 4], c.S[X][0][e >> 4][e & 15]); }
    }
    if constexpr (e_lo + 0 < e_hi) c.S[X][0][(e_lo + 0) >> 4][(e_lo + 0) & 15] = __builtin_amdgcn_exp2f(c.S[X][0][(e_lo + 0) >> 4][(e_lo + 0) & 15]);
    if constexpr (e_lo + 1 < e_hi) c.S[X][0][(e_lo + 1) >> 4][(e_lo + 1) & 15] = __builtin_amdgcn_exp2f(c.S[X][0][(e_lo + 1) >> 4][(e_lo + 1) & 15]);
    if constexpr (e_lo + 2 < e_hi) c.S[X][0][(e_lo + 2) >> 4][(e_lo + 2) & 15] = __builtin_amdgcn_exp2f(c.S[X][0][(e_lo + 2) >> 4][(e_lo + 2) & 15]);
  }
  if constexpr (M == 31) asm volatile("" :: "v"(c.S[X][0][0]), "v"(c.S[X][0][1]), "v"(c.NEGM[0]), "v"(c.NEGM[1]), "v"(c.rs0[0]), "v"(c.rs0[1]), "v"(c.rs1[0]), "v"(c.rs1[1]));
  GAP();
}
template <int R> DI void o_scale1(float f0, float f1) {
  float t; asm volatile("v_accvgpr_read_b32 %0, a[%c2]\n\tv_mul_f32 %0, %1, %0\n\tv_accvgpr_write_b32 a[%c2], %0" : "=&v"(t) : "v"(((R >> 4) & 1) ? f1 : f0), "i"(R) : ACLOB_LO);
}
template <int... Rs> DI void o_scale(float f0, float f1, iseq<Rs...>) { (o_scale1<Rs>(f0, f1), ...); }
template <int X, bool PEEL, bool ZERO1, bool DK, bool DV, bool KR, int SJ, int... Ms> DI void phaseB_(W4C& c, const int t, iseq<Ms...>) {
  const unsigned kslot_rd = (unsigned)((t + 1) & 1) * 0x4000u;
  if constexpr (!PEEL) asm volatile("s_nop 1" ::: "memory");
  GAP();
  (gapB<X, PEEL, ZERO1, DK, DV, KR, SJ, Ms>(c, kslot_rd), ...);
#pragma unroll
  for (int e = (32 * 12) / 13; e < 32; ++e) { if (e & 1) c.rs1[e >> 4] = fadd_s(c.rs1[e >> 4], c.S[X][0][e >> 4][e & 15]); else c.rs0[e >> 4] = fadd_s(c.rs0[e >> 4], c.S[X][0][e >> 4][e & 15]); }
  if constexpr (!PEEL) {
    if (__builtin_expect(c.pend, 0)) {
      asm volatile("s_nop 7\n\ts_nop 7" ::: "memory");
      o_scale(c.fct[0], c.fct[1], make_iseq<128>{});
      asm volatile("s_nop 1" ::: "memory");
      c.lsum[0] = fmul_s(c.lsum[0], c.fct[0]); c.lsum[1] = fmul_s(c.lsum[1], c.fct[1]); c.pend = false;
    }
  }
  asm volatile("s_waitcnt lgkmcnt(0)" ::: "memory");
  GAP();
}
template <int X, bool PEEL, bool ZERO1, bool DK = true, bool DV = true, bool KR = true, int SJ = 0> DI void phaseB(W4C& c, const int t) { phaseB_<X, PEEL, ZERO1, DK, DV, KR, SJ>(c, t, make_iseq<32>{}); }
template <int... Is> DI void tr_reads(W4C& c, unsigned vslot, iseq<Is...>) { (tr_read<Is>(c, vslot), ...); }
template <int X, int... Cs> DI void packs(W4C& c, iseq<Cs...>) { (pack1<X, ((Cs >> 3) & 1), 2 * (Cs >> 4) + ((Cs >> 2) & 1), (Cs & 3)>(c), ...); }
template <int X, int J> DI void rowsum1_padded(W4C& c) { rowsum1<X, J>(c); asm volatile("s_nop 1"); }
template <int X, int... Js> DI void rowsums(W4C& c, iseq<Js...>) { (rowsum1_padded<X, Js>(c), ...); }
template <int... Ms> DI void pv_all(W4C& c, iseq<Ms...>) { (pv_mfma<Ms, false>(c), ...); }

template <int X, int KB, int E> DI void f_exp(W4C& c) { constexpr int qb = KB ? (E >> 4) : ((E >> 3) & 1), r = KB ? (E & 15) : 8 * (E >> 4) + (E & 7); c.S[X][KB][qb][r] = __builtin_amdgcn_exp2f(c.S[X][KB][qb][r]); }
template <int X, int KB, int I> DI void f_add(W4C& c) { constexpr int qb = KB ? (I >> 3) : ((I >> 2) & 1), r = KB ? 2 * (I & 7) : 8 * (I >> 3) + 2 * (I & 3);
  c.rs0[qb] = fadd_s(c.rs0[qb], c.S[X][KB][qb][r]); c.rs1[qb] = fadd_s(c.rs1[qb], c.S[X][KB][qb][r + 1]); }
template <int X, int KB, int I> DI void f_pack(W4C& c) { pack1<X, (KB ? (I >> 3) : ((I >> 2) & 1)), 2 * KB + (KB ? ((I >> 2) & 1) : (I >> 3)), (I & 3)>(c); }
template <int SL, bool FIRST, int M> DI void gapAf(W4C& c, const unsigned tk, const unsigned tv) {
  constexpr int X = SL & 1, Xp = X ^ 1, VS = ((SL + 3) & 3) * 0x4000;
  qk_mfma<X, FIRST, M>(c);
  if constexpr (M >= 16) {
    constexpr int q = (M - 16) >> 1;
    if constexpr (((M - 16) & 1) == 0) {
      if constexpr (q < 4) c.pm0[q] = c.m0k + SL * 0x4000u + 1024u * q; else c.pm0[q] = c.m0v + VS + 1024u * (q - 4);
      asm volatile("" : "+s"(c.pm0[q]));
    } else {
      if constexpr (q < 4) c.psf[q] = tk + KRS * 4u * q; else c.psf[q] = tv + KRS * 8u * ((q - 4) >> 1) + 128u * ((q - 4) & 1);
      asm volatile("" : "+s"(c.psf[q]));
    }
  }
  if constexpr (!FIRST) {
    if constexpr (M == 8) asm volatile("" :: "v"(c.NEGM[0]), "v"(c.NEGM[1]));
    if constexpr (M < 12) { tr_read<(3 * M) / 2, VS>(c, 0u); if constexpr (M & 1) tr_read<(3 * M) / 2 + 1, VS>(c, 0u); }
    else if constexpr (M < 26) tr_read<M + 6, VS>(c, 0u);
    if constexpr (M == 0) { f_add<Xp, 0, 15>(c); f_pack<Xp, 0, 15>(c); }
    f_exp<Xp, 1, M>(c);
    if constexpr (M >= 2 && (M & 1) == 0) f_add<Xp, 1, (M - 2) / 2>(c);
    if constexpr (M >= 3 && (M & 1) == 1) f_pack<Xp, 1, (M - 3) / 2>(c);
  }
  GAP();
}
template <int SL, bool FIRST, int VMA, int... Ms> DI void phaseAf_(W4C& c, const int t, iseq<Ms...>) {
  const unsigned tk = KTILEB * (unsigned)(t + 4 < NTILE ? t + 4 : NTILE - 1) + c.sofk, tv = KTILEB * (unsigned)(t + 3 < NTILE ? t + 3 : NTILE - 1) + c.sofv;
  (gapAf<SL, FIRST, Ms>(c, tk, tv), ...);
  if constexpr (FIRST) asm volatile("s_waitcnt vmcnt(%c0) lgkmcnt(0)\n\ts_barrier" :: "i"(VMA) : "memory");
  else { asm volatile("s_waitcnt vmcnt(%c16) lgkmcnt(0)" : W4_VLO : "i"(VMA) : "memory"); asm volatile("s_barrier" : W4_VUP :: "memory"); }
  GAP();
  if constexpr (!FIRST) v_concat(c);
}
template <int SL, int VMA, bool FIRST = false> DI void phaseAf(W4C& c, const int t) { phaseAf_<SL, FIRST, VMA>(c, t, make_iseq<32>{}); }
template <int SL, bool PEEL, bool ZERO1, bool DM_, bool KR, bool SJ, int M> DI void gapBf(W4C& c) {
  constexpr int X = SL & 1;
  if constexpr (!PEEL) pv_mfma<M, ZERO1>(c);
  if constexpr (M < 4 && DM_) dma16(c.pm0[M], c.vksrc[M & 1], c.srdK, c.psf[M]);
  else if constexpr (M >= 4 && M < 8 && DM_) dma16(c.pm0[M], c.vvsrc, c.srdV, c.psf[M]);
  if constexpr (SJ && M == 24) asm volatile("buffer_store_dwordx4 %0, %1, %2, %3 offen\n\ts_nop 1" : "+v"(c.sw[0]) : "v"(c.vss), "s"(c.srdSS), "s"(c.sss) : "memory");
  if constexpr (SJ && M == 26) asm volatile("buffer_store_dwordx4 %0, %1, %2, %3 offen\n\ts_nop 1" : "+v"(c.sw[1]) : "v"(c.vss), "s"(c.srdSS), "s"(c.sss + 65536u) : "memory");
  if constexpr (SJ && M == 28) asm volatile("buffer_load_dwordx4 %0, %1, %2, %3 offen" : "=v"(c.sw[0]) : "v"(c.vsl), "s"(c.srdSL), "s"(c.ssl) : "memory");
  if constexpr (SJ && M == 30) asm volatile("buffer_load_dwordx4 %0, %1, %2, %3 offen offset:32" : "=v"(c.sw[1]) : "v"(c.vsl), "s"(c.srdSL), "s"(c.ssl) : "memory");
  if constexpr (M >= 8 && M < 24 && KR) k_read<M - 8, ((SL + 1) & 3) * 0x4000>(c, 0u);
  if constexpr (!PEEL && M == 9) { f_add<X ^ 1, 1, 15>(c); f_pack<X ^ 1, 1, 15>(c); }
  f_exp<X, 0, M>(c);
  if constexpr (M >= 2 && (M & 1) == 0) f_add<X, 0, (M - 2) / 2>(c);
  if constexpr (M >= 17) f_pack<X, 0, M - 17>(c);
  GAP();
}
template <int SL, bool PEEL, bool ZERO1, bool DM_, bool KR, bool SJ, int... Ms> DI void phaseBf_(W4C& c, iseq<Ms...>) {
  constexpr int X = SL & 1;
  if constexpr (PEEL) {
    asm volatile("s_nop 7\n\ts_nop 7" ::: "memory");
#pragma unroll
    for (int q = 0; q < 2; ++q) {
      float m0 = max2f(c.S[X][0][q][0], c.S[X][1][q][0]);
#pragma unroll
      for (int e = 1; e < 16; ++e) m0 = max3f(m0, c.S[X][0][q][e], c.S[X][1][q][e]);
      auto pr = __builtin_amdgcn_permlane32_swap(__float_as_uint(m0), __float_as_uint(m0), false, false);
      const float mx = max2f(__uint_as_float(pr[0]), __uint_as_float(pr[1]));
#pragma unroll
      for (int kb = 0; kb < 2; ++kb) { _Pragma("unroll") for (int e = 0; e < 16; ++e) c.S[X][kb][q][e] = fsub_s(c.S[X][kb][q][e], mx); }
      negm_bcast(c.NEGM[q], w4_aone(), -mx);
      c.rs0[q] = 0.f; c.rs1[q] = 0.f;
    }
  } else asm volatile("s_nop 1" ::: "memory");
  GAP();
  (gapBf<SL, PEEL, ZERO1, DM_, KR, SJ, Ms>(c), ...);
  asm volatile("s_waitcnt lgkmcnt(0)" ::: "memory");
  GAP();
}
template <int SL, bool PEEL = false, bool ZERO1 = false, bool DM_ = true, bool KR = true, bool SJ = false> DI void phaseBf(W4C& c) { phaseBf_<SL, PEEL, ZERO1, DM_, KR, SJ>(c, make_iseq<32>{}); }
template <int TI, int I> DI void kv_k(W4C& c) { dma16(c.m0k + TI * 0x4000u + 1024u * I, c.vksrc[I & 1], c.srdK, TI * KTILEB + c.sofk + KRS * 4u * I); }
template <int TI, int I> DI void kv_v(W4C& c) { dma16(c.m0v + TI * 0x4000u + 1024u * I, c.vvsrc, c.srdV, TI * KTILEB + c.sofv + KRS * 8u * (I >> 1) + 128u * (I & 1)); }
template <int X, int... Es> DI void f_exps(W4C& c, iseq<Es...>) { (f_exp<X, 1, Es>(c), ...); }
template <int X, int... Is> DI void f_fin(W4C& c, iseq<Is...>) { ((f_add<X, 1, Is>(c), f_pack<X, 1, Is>(c)), ...); }
template <int... Is> DI void k_reads(W4C& c, iseq<Is...>) { (k_read<Is>(c, 0u), ...); }

struct QPC { unsigned vtr, vwsrc, m0w, sofw, vx; rsrc_t srdW, srdX; bf16x4 wl[8], wu[8]; u32x4 xv[4][8]; };
template <int F> DI void qp_tr(QPC& q, unsigned vaddr) {
  constexpr int ks = F >> 2, db = F & 3;
  asm volatile("ds_read_b64_tr_b16 %0, %1 offset:%2" : "=v"(q.wl[F & 7]) : "v"(vaddr), "i"(4096 * ks + 512 * db) : "memory");
  asm volatile("ds_read_b64_tr_b16 %0, %1 offset:%2" : "=v"(q.wu[F & 7]) : "v"(vaddr), "i"(4096 * ks + 256 + 512 * db) : "memory");
}
template <int T, int Q> DI void qp_g(QPC& q) {
  if constexpr (Q < 4) dma16(q.m0w + T * 0x4000u + 1024u * Q, q.vwsrc, q.srdW, q.sofw + T * 64u * 8192u + 8192u * 8u * (Q >> 1) + 128u * (Q & 1));
  else {
    constexpr int J = Q - 4, a = 128 + 32 * (T & 3) + 4 * J;
    if constexpr (T >= 4 && T < 8) asm volatile("buffer_load_dwordx4 %0, %1, %2, %3 offen offset:%c4" : "=v"(q.xv[T & 3][J]) : "v"(q.vx), "s"(q.srdX), "s"((unsigned)((4 * T + (J & 3)) * 65536)), "i"((J >> 2) * 1024) : "memory");
    else asm volatile("buffer_load_dwordx4 a[%c3:%c4], %0, %1, %2 offen offset:%c5" :: "v"(q.vx), "s"(q.srdX), "s"((unsigned)((4 * T + (J & 3)) * 65536)), "i"(a), "i"(a + 3), "i"((J >> 2) * 1024) : "memory", ACLOB_HI);
  }
}
template <int T, int... Q> DI void qp_grp(QPC& q, iseq<Q...>) { (qp_g<T, Q>(q), ...); }
template <int TT, int M, int LG> DI void qp_mfma(QPC& q) {
  constexpr int f = M >> 1, ks = f >> 2, db = f & 3, qb = M & 1, ao = AO_(db, qb), ax = 128 + 32 * (TT & 3) + 16 * qb + 4 * ks;
  if constexpr (qb == 0) asm volatile("s_waitcnt lgkmcnt(%c2)" : "+v"(q.wl[f & 7]), "+v"(q.wu[f & 7]) : "i"(LG) : "memory");
  const bf16x8 wf = __builtin_shufflevector(q.wl[f & 7], q.wu[f & 7], 0, 1, 2, 3, 4, 5, 6, 7);
  if constexpr (TT == 0 && ks == 0) asm volatile("v_mfma_f32_32x32x16_bf16 a[%c1:%c2], %0, a[%c3:%c4], 0" :: "v"(wf), "i"(ao), "i"(ao + 15), "i"(ax), "i"(ax + 3) : ACLOB);
  else if constexpr (TT >= 4 && TT < 8) asm volatile("v_mfma_f32_32x32x16_bf16 a[%c2:%c3], %0, %1, a[%c2:%c3]" :: "v"(wf), "v"(q.xv[TT & 3][4 * qb + ks]), "i"(ao), "i"(ao + 15) : ACLOB);
  else asm volatile("v_mfma_f32_32x32x16_bf16 a[%c1:%c2], %0, a[%c3:%c4], a[%c1:%c2]" :: "v"(wf), "i"(ao), "i"(ao + 15), "i"(ax), "i"(ax + 3) : ACLOB);
}
template <int T, int J> DI void qp_gap(QPC& q, W4C& c, unsigned vrd) {
  if constexpr (J < 8) { if constexpr (T >= 0) qp_mfma<T, 24 + J, (T == 9 ? 0 : 6)>(q); }
  else qp_mfma<T + 1, J - 8, 6>(q);
  if constexpr (T < 9 && (J & 1) == 0) qp_tr<(J >> 1)>(q, vrd);
  if constexpr (T <= 1 && J < 24) qp_g<2 * T + 4 + J / 12, J % 12>(q);
  if constexpr ((T == 2 || T == 3) && (J & 1) && J < 24) qp_g<T + 6, (J >> 1)>(q);
  if constexpr (T == 4 && (J & 1) && J < 24) kv_k<(J >> 3), (J >> 1) & 3>(c);
  if constexpr (T == 5 && (J & 1) && J < 8) kv_k<3, (J >> 1)>(c);
  if constexpr (T >= 5 && T <= 7 && (J & 1) && J >= 8 && J < 16) kv_v<T - 5, (J >> 1) & 3>(c);
  if constexpr (T == 9 && J >= 6) { k_read<8 * (J - 6)>(c, 0u); k_read<8 * (J - 6) + 1>(c, 0u); k_read<8 * (J - 6) + 2>(c, 0u); k_read<8 * (J - 6) + 3>(c, 0u);
                                    k_read<8 * (J - 6) + 4>(c, 0u); k_read<8 * (J - 6) + 5>(c, 0u); k_read<8 * (J - 6) + 6>(c, 0u); k_read<8 * (J - 6) + 7>(c, 0u); }
  GAP();
}
template <int T, int VM, int... Js> DI void qp_step_(QPC& q, W4C& c, iseq<Js...>) {
  const unsigned vrd = q.vtr + (T + 1) * 0x4000u;
  constexpr int u = (T + 1) & 3;
  if constexpr (T + 1 >= 4 && T + 1 < 8) asm volatile("s_waitcnt vmcnt(%c8)\n\ts_barrier" : "+v"(q.xv[u][0]), "+v"(q.xv[u][1]), "+v"(q.xv[u][2]), "+v"(q.xv[u][3]), "+v"(q.xv[u][4]), "+v"(q.xv[u][5]), "+v"(q.xv[u][6]), "+v"(q.xv[u][7]) : "i"(VM) : "memory");
  else if constexpr (T < 9) asm volatile("s_waitcnt vmcnt(%c0)\n\ts_barrier" :: "i"(VM) : "memory");
  GAP();
  (qp_gap<T, Js>(q, c, vrd), ...);
}
template <int T, int VM> DI void qp_step(QPC& q, W4C& c) { qp_step_<T, VM>(q, c, make_iseq<(T == 9 ? 8 : 32)>{}); }
template <int R> DI u32x4 acc_pack8(float f, float* chk = nullptr) {
  float o[8];
  asm volatile("v_accvgpr_read_b32 %0, a[%c8]\n\tv_accvgpr_read_b32 %1, a[%c9]\n\tv_accvgpr_read_b32 %2, a[%c10]\n\tv_accvgpr_read_b32 %3, a[%c11]\n\t"
               "v_accvgpr_read_b32 %4, a[%c12]\n\tv_accvgpr_read_b32 %5, a[%c13]\n\tv_accvgpr_read_b32 %6, a[%c14]\n\tv_accvgpr_read_b32 %7, a[%c15]"
               : "=v"(o[0]), "=v"(o[1]), "=v"(o[2]), "=v"(o[3]), "=v"(o[4]), "=v"(o[5]), "=v"(o[6]), "=v"(o[7])
               : "i"(R), "i"(R + 1), "i"(R + 2), "i"(R + 3), "i"(R + 4), "i"(R + 5), "i"(R + 6), "i"(R + 7));
  u32x4 w;
#pragma unroll
  for (int e = 0; e < 4; ++e) w[e] = cvtpk(o[2 * e] * f, o[2 * e + 1] * f);
  if (chk) {
#pragma unroll
    for (int e = 0; e < 8; ++e) asm("v_fmac_f32_e32 %0, 0, %1" : "+v"(chk[e & 1]) : "v"(o[e]));
  }
  return w;
}
template <int I> DI void q_conv1(float C) {
  constexpr int qb = I >> 3, d0 = I & 7, aq = AQ_(qb, d0);
  const u32x4 w = acc_pack8<AO_(d0 >> 1, qb) + 8 * (d0 & 1)>(C);
  asm volatile("v_accvgpr_write_b32 a[%c4], %0\n\tv_accvgpr_write_b32 a[%c5], %1\n\tv_accvgpr_write_b32 a[%c6], %2\n\tv_accvgpr_write_b32 a[%c7], %3"
               :: "v"(w[0]), "v"(w[1]), "v"(w[2]), "v"(w[3]), "i"(aq), "i"(aq + 1), "i"(aq + 2), "i"(aq + 3) : ACLOB_HI);
}
template <int... Is> DI void q_conv(float C, iseq<Is...>) { (q_conv1<Is>(C), ...); }
template <int I> DI void o_store1(unsigned char* dst, float il0, float il1, float* chk) {
  constexpr int qb = I >> 3, j = I & 7;
  const u32x4 w = acc_pack8<AO_(j >> 1, qb) + 8 * (j & 1)>(qb ? il1 : il0, chk);
  *(u32x4*)(dst + qb * 1024u + (size_t)j * 65536u) = w;
}
template <int... Is> DI void o_store(unsigned char* dst, float il0, float il1, float* chk, iseq<Is...>) { (o_store1<Is>(dst, il0, il1, chk), ...); }
DI void gload16(u32x4& r, const void* p) { asm volatile("global_load_dwordx4 %0, %1, off" : "=v"(r) : "v"(p) : "memory"); }
DI void gstore16(void* p, const u32x4& r) { asm volatile("global_store_dwordx4 %0, %1, off\n\ts_nop 1" :: "v"(p), "v"(r) : "memory"); }

__global__ __launch_bounds__(256, 1)
void k_attn(const unsigned char* __restrict__ xnF, const unsigned short* __restrict__ wq, const unsigned short* __restrict__ Kc, const unsigned short* __restrict__ Vc,
            unsigned char* __restrict__ OF, const unsigned char* __restrict__ zF, unsigned char* __restrict__ z2nT,
            const unsigned short* __restrict__ w_sp, unsigned char* __restrict__ wspF,
            const unsigned short* __restrict__ wo, const unsigned short* __restrict__ w_out) {
  extern __shared__ __attribute__((aligned(16))) char lds[];
  const unsigned tid = threadIdx.x, lane = tid & 63u, wid = rfl(tid >> 6), l32 = lane & 31u, hi = lane >> 5;
  constexpr int T = NTILE;
  const float C = 0.12751743082f;
  const unsigned xcd = blockIdx.x & 7u, idx = blockIdx.x >> 3;
  const unsigned h = 8u * (xcd >> 1) + (idx & 7u), y = 4u * (xcd & 1u) + (idx >> 3);
  const unsigned rb0 = 8u * y + 2u * wid;
  W4C c; QPC q;
  TS(2, 0);
  c.ldsb = (unsigned)(size_t)lds;
#pragma unroll
  for (int j = 0; j < 4; ++j) c.vkb[j] = c.ldsb + KRING + 256u * l32 + 16u * ((2u * j + hi) ^ (l32 & 7u));
  c.vvb = c.ldsb + VRING + tr_lane<false, false>(lane, 0u);
#pragma unroll
  for (int par = 0; par < 2; ++par) c.vksrc[par] = KRS * (lane >> 4) + 16u * ((lane & 15u) ^ (4u * par + (lane >> 4)));
  c.vvsrc = KRS * ((lane >> 2) & 7u) + 64u * hi + 16u * (lane & 3u);
  c.m0k = rfl(c.ldsb + KRING + 4096u * wid); c.m0v = rfl(c.ldsb + VRING + 4096u * wid);
  c.sofk = KRS * 16u * wid; c.sofv = KRS * 16u * wid;
  c.srdK = mk_srd((const char*)Kc + (size_t)h * SEQ * 256u, SEQ * 256u);
  c.srdV = mk_srd((const char*)Vc + (size_t)h * SEQ * 256u, SEQ * 256u);
  q.vtr = c.ldsb + WRING + tr_lane<true, true>(lane, 2048u);
  q.vwsrc = 8192u * ((lane >> 2) & 7u) + 64u * hi + 16u * (lane & 3u);
  q.m0w = rfl(c.ldsb + WRING + 4096u * wid); q.sofw = 8192u * 16u * wid;
  q.vx = rb0 * 1024u + lane * 16u;
  q.srdW = mk_srd((const char*)wq + h * 256u, DM * 8192u - h * 256u);
  q.srdX = mk_srd(xnF, SEQ * DM * 2u);
  const unsigned srow = blockIdx.x * 8u + 2u * wid + hi;
  u32x4 zv[4];
#pragma unroll
  for (int i = 0; i < 4; ++i) { const unsigned pc = l32 + 32u * i, p = pc < 108u ? pc : 107u;
    gload16(zv[i], zF + ((size_t)((54u + (p >> 1)) * 64u + (srow >> 5)) * 64u + (p & 1u) * 32u + (srow & 31u)) * 16u); }
  asm volatile("s_nop 4" ::: "memory");
  qp_grp<0>(q, make_iseq<12>{}); qp_grp<1>(q, make_iseq<12>{});
  asm volatile("s_waitcnt vmcnt(24)" : "+v"(zv[0]), "+v"(zv[1]), "+v"(zv[2]), "+v"(zv[3]) :: "memory");
  {
    const bool v3 = l32 < 12u;
    float f[32], s = 0.f;
#pragma unroll
    for (int i = 0; i < 4; ++i)
#pragma unroll
      for (int e = 0; e < 4; ++e) { f[8 * i + 2 * e] = bflo(zv[i][e]); f[8 * i + 2 * e + 1] = bfhi(zv[i][e]); const float t2 = f[8 * i + 2 * e] + f[8 * i + 2 * e + 1]; s += (i < 3 || v3) ? t2 : 0.f; }
#pragma unroll
    for (int o = 16; o; o >>= 1) s += __shfl_xor(s, o);
    const float mean = s * (1.f / FH); float qq = 0.f;
#pragma unroll
    for (int i = 0; i < 32; ++i) { f[i] -= mean; qq += (i < 24 || v3) ? f[i] * f[i] : 0.f; }
#pragma unroll
    for (int o = 16; o; o >>= 1) qq += __shfl_xor(qq, o);
    const float rstd = 1.f / sqrtf(qq * (1.f / FH) + 1e-5f);
#pragma unroll
    for (int i = 0; i < 4; ++i) { const unsigned pc = l32 + 32u * i, p = pc < 108u ? pc : 107u;
      u32x4 w;
#pragma unroll
      for (int e = 0; e < 4; ++e) w[e] = cvtpk(f[8 * i + 2 * e] * rstd, f[8 * i + 2 * e + 1] * rstd);
      gstore16(z2nT + ((size_t)(blockIdx.x * 27u + (p >> 2)) * 8u + (srow & 7u)) * 64u + (p & 3u) * 16u, w); }
  }
  TS(2, 4); TS(2, 5);
  asm volatile("s_waitcnt lgkmcnt(0)" ::: "memory");
  qp_step<-1, 16>(q, c); qp_step<0, 28>(q, c); qp_step<1, 36>(q, c); qp_step<2, 48>(q, c); qp_step<3, 48>(q, c); qp_step<4, 48>(q, c);
  qp_step<5, 48>(q, c); qp_step<6, 44>(q, c); qp_step<7, 36>(q, c); qp_step<8, 24>(q, c); qp_step<9, 0>(q, c);
  asm volatile("s_nop 7\n\ts_nop 7" ::: "memory");
  q_conv(C, make_iseq<16>{});
  asm volatile("s_waitcnt lgkmcnt(0)\n\ts_nop 1" ::: "memory");
  GAP();
  TS(2, 1);
  { const unsigned srb = blockIdx.x >> 2, ks0 = (blockIdx.x & 3u) * 32u + wid * 8u;
    c.vsl = (srb * 32u + l32) * 4096u + ks0 * 32u + hi * 16u; c.vss = (ks0 * 64u + srb) * 1024u + lane * 16u;
    c.srdSL = mk_srd(w_sp, SEQ * SEQ * 2u); c.srdSS = mk_srd(wspF, SEQ * SEQ * 2u); c.sw[0] = c.sw[1] = (u32x4){0u, 0u, 0u, 0u}; }
  phaseAf<0, 8, true>(c, 0); phaseBf<0, true>(c);
  phaseAf<1, 12>(c, 1); phaseBf<1, false, true>(c);
  { const unsigned gw = blockIdx.x * 4u + wid;
    const bool isw = gw < 640u; const unsigned go = isw ? gw : (gw < 775u ? gw - 640u : 134u);
    const rsrc_t srdP = mk_srd(isw ? wo : w_out, isw ? NH * DH * DM * 2u : FH * DM * 2u);
    asm volatile("s_mov_b32 m0, %0\n\ts_nop 4\n\tbuffer_load_dword %1, %2, %3 offen lds\n\tbuffer_load_dword %1, %2, %3 offen offset:64 lds"
                 :: "s"(c.ldsb + 0x20000u + wid * 1024u), "v"(lane * 128u), "s"(srdP), "s"(go * 8192u) : "m0", "memory"); }
  for (int t = 2; t < T - 2; t += 4) {
    const unsigned fi = (unsigned)(t >> 2), fl = fi < 3u ? fi : 3u, fs = fi == 0u ? 0u : (fi - 1u < 3u ? fi - 1u : 3u);
    c.ssl = fl * 64u; c.sss = fs * 131072u;
    phaseAf<2, 16>(c, t);     phaseBf<2, false, false, true, true, true>(c);
    phaseAf<3, 20>(c, t + 1); phaseBf<3>(c);
    phaseAf<0, 20>(c, t + 2); phaseBf<0>(c);
    phaseAf<1, 20>(c, t + 3); phaseBf<1>(c);
  }
  TS(2, 2);
  phaseAf<2, 16>(c, T - 2); phaseBf<2, false, false, false, true>(c);
  phaseAf<3, 0>(c, T - 1);  phaseBf<3, false, false, false, false>(c);
  asm volatile("" :: "v"(c.sw[0]), "v"(c.sw[1]));
  unsigned lb; asm volatile("v_mbcnt_lo_u32_b32 %0, -1, 0\n\tv_mbcnt_hi_u32_b32 %0, -1, %0" : "=v"(lb));
  unsigned char* const odst = OF + ((size_t)(8u * h * 64u + rb0) * 64u + lb) * 16u;
  bool bad;
  {
    tr_reads(c, 3u * 0x4000u, make_iseq<32>{});
    f_add<1, 0, 15>(c); f_pack<1, 0, 15>(c);
    f_exps<1>(c, make_iseq<32>{});
    f_fin<1>(c, make_iseq<16>{});
    asm volatile("s_waitcnt lgkmcnt(0)" : W4_VLO :: "memory"); asm volatile("" : W4_VUP :: "memory");
    GAP();
    v_concat(c);
    asm volatile("s_nop 1" ::: "memory");
    pv_all(c, make_iseq<32>{});
    float l[2];
#pragma unroll
    for (int qb = 0; qb < 2; ++qb) { const float rs = c.rs0[qb] + c.rs1[qb];
      auto pr = __builtin_amdgcn_permlane32_swap(__float_as_uint(rs), __float_as_uint(rs), false, false); l[qb] = __uint_as_float(pr[0]) + __uint_as_float(pr[1]); }
    asm volatile("s_nop 7\n\ts_nop 7" ::: "memory");
    float chk[2] = {0.f, 0.f};
    o_store(odst, __builtin_amdgcn_rcpf(l[0]), __builtin_amdgcn_rcpf(l[1]), chk, make_iseq<16>{});
    const bool ok = (l[0] < 0x1p64f) & (l[1] < 0x1p64f) & (chk[0] == 0.f) & (chk[1] == 0.f);
    volatile unsigned* flag = (volatile unsigned*)(lds + 0x22000);
    const unsigned wbad = __any((int)!ok) ? 1u : 0u;
    if (lb == 0u) flag[wid] = wbad;
    __syncthreads();
    bad = rfl(flag[0] | flag[1] | flag[2] | flag[3]) != 0u;
  }
  if (__builtin_expect(bad, 0)) {
    asm volatile("s_waitcnt vmcnt(0) lgkmcnt(0)\n\ts_nop 4" ::: "memory");
#pragma unroll
    for (int i = 0; i < 4; ++i) {
      dma16(c.m0k + 1024u * i, c.vksrc[i & 1], c.srdK, c.sofk + KRS * 4u * i);
      dma16(c.m0v + 1024u * i, c.vvsrc, c.srdV, c.sofv + KRS * 8u * (i >> 1) + 128u * (i & 1));
      dma16(c.m0k + 0x4000u + 1024u * i, c.vksrc[i & 1], c.srdK, KTILEB + c.sofk + KRS * 4u * i);
    }
    asm volatile("s_waitcnt vmcnt(0)\n\ts_barrier" ::: "memory");
    k_reads(c, make_iseq<16>{});
    asm volatile("s_waitcnt lgkmcnt(0)" ::: "memory");
    GAP();
    c.mhat[0] = c.mhat[1] = 0.f; c.lsum[0] = c.lsum[1] = 0.f; c.fct[0] = c.fct[1] = 1.f; c.pend = false;
    phaseA<0, true>(c, 0);  phaseB<0, true, false>(c, 0);
    phaseA<1, false>(c, 1); phaseB<1, false, true>(c, 1);
    for (int t = 2; t < T - 2; t += 2) {
      phaseA<0, false>(c, t);     phaseB<0, false, false>(c, t);
      phaseA<1, false>(c, t + 1); phaseB<1, false, false>(c, t + 1);
    }
    phaseA<0, false>(c, T - 2); phaseB<0, false, false, false, true, true>(c, T - 2);
    phaseA<1, false>(c, T - 1); phaseB<1, false, false, false, false, false>(c, T - 1);
    tr_reads(c, (unsigned)((T - 1) & 1) * 0x4000u, make_iseq<32>{});
#pragma unroll
    for (int e = 0; e < 32; ++e) c.S[1][1][e >> 4][e & 15] = __builtin_amdgcn_exp2f(c.S[1][1][e >> 4][e & 15]);
    packs<1>(c, make_iseq<32>{});
    rowsums<1>(c, iseq<8, 9, 10, 11, 12, 13, 14, 15, 24, 25, 26, 27, 28, 29, 30, 31>{});
    lsum_merge(c);
    asm volatile("s_waitcnt lgkmcnt(0)" : W4_VLO :: "memory"); asm volatile("" : W4_VUP :: "memory");
    GAP();
    v_concat(c);
    asm volatile("s_nop 1" ::: "memory");
    pv_all(c, make_iseq<32>{});
    asm volatile("s_nop 7\n\ts_nop 7" ::: "memory");
    unsigned lb2; asm volatile("v_mbcnt_lo_u32_b32 %0, -1, 0\n\tv_mbcnt_hi_u32_b32 %0, -1, %0" : "=v"(lb2));
    o_store(OF + ((size_t)(8u * h * 64u + rb0) * 64u + lb2) * 16u, __builtin_amdgcn_rcpf(c.lsum[0]), __builtin_amdgcn_rcpf(c.lsum[1]), nullptr, make_iseq<16>{});
  }
  TS(2, 3);
}

extern "C" void kernel_launch(void* const* d_in, const int* in_sizes, int n_in, void* d_out, int out_size, void* d_ws, size_t ws_size, hipStream_t stream) {
  const unsigned short* x = (const unsigned short*)d_in[0];
  const unsigned short* Kc = (const unsigned short*)d_in[1];
  const unsigned short* Vc = (const unsigned short*)d_in[2];
  const unsigned short* wq = (const unsigned short*)d_in[3];
  const unsigned short* wo = (const unsigned short*)d_in[4];
  const unsigned short* w_in = (const unsigned short*)d_in[6];
  const unsigned short* w_sp = (const unsigned short*)d_in[7];
  const unsigned short* b_sp = (const unsigned short*)d_in[8];
  const unsigned short* w_out = (const unsigned short*)d_in[9];
  unsigned char* ws = (unsigned char*)d_ws;
  (void)hipFuncSetAttribute((const void*)k_attn, hipFuncAttributeMaxDynamicSharedMemorySize, 163840);
  (void)hipFuncSetAttribute((const void*)k_zgemm, hipFuncAttributeMaxDynamicSharedMemorySize, 163840);
  (void)hipFuncSetAttribute((const void*)k_mid, hipFuncAttributeMaxDynamicSharedMemorySize, 163840);
  (void)hipFuncSetAttribute((const void*)k_out, hipFuncAttributeMaxDynamicSharedMemorySize, 110592);
  hipLaunchKernelGGL(k_ln, dim3(484), dim3(256), 0, stream, x, ws + WS_XN, w_in, wq);
  hipLaunchKernelGGL(k_zgemm, dim3(224), dim3(256), 163840, stream, ws + WS_XN, w_in, ws + WS_Z);
  hipLaunchKernelGGL(k_attn, dim3(256), dim3(256), 163840, stream, ws + WS_XN, wq, Kc, Vc, ws + WS_O, ws + WS_Z, ws + WS_Z2N, w_sp, ws + WS_WSP, wo, w_out);
  hipLaunchKernelGGL(k_mid, dim3(256), dim3(256), 163840, stream, ws + WS_O, wo, ws + WS_SP, ws + WS_WSP, ws + WS_Z2N, ws + WS_Z, b_sp, ws + WS_U);
  hipLaunchKernelGGL(k_out, dim3(160), dim3(256), 110592, stream, ws + WS_U, w_out, ws + WS_SP, x, (unsigned short*)d_out);
}
```

```cpp
#include <hip/hip_runtime.h>
#include <hip/hip_bf16.h>
#include <utility>

typedef float    f32x16 __attribute__((ext_vector_type(16)));
typedef float    f32x2  __attribute__((ext_vector_type(2)));
typedef float    f32x4  __attribute__((ext_vector_type(4)));
typedef __bf16   bf16x8 __attribute__((ext_vector_type(8)));
typedef __bf16   bf16x4 __attribute__((ext_vector_type(4)));
typedef __bf16   bf16x2 __attribute__((ext_vector_type(2)));
typedef unsigned u32x4  __attribute__((ext_vector_type(4)));
typedef unsigned u32x2  __attribute__((ext_vector_type(2)));
template <int... Is> using iseq = std::integer_sequence<int, Is...>;
template <int N> using make_iseq = std::make_integer_sequence<int, N>;
typedef __amdgpu_buffer_rsrc_t rsrc_t;

#define DI __device__ __forceinline__
#define GAP() __builtin_amdgcn_sched_barrier(0)
#define SRD_WORD3 0x00020000
#define A10(x) "a" #x "0", "a" #x "1", "a" #x "2", "a" #x "3", "a" #x "4", "a" #x "5", "a" #x "6", "a" #x "7", "a" #x "8", "a" #x "9"
#define ACLOB_LO "a0", "a1", "a2", "a3", "a4", "a5", "a6", "a7", "a8", "a9", A10(1), A10(2), A10(3), A10(4), A10(5), A10(6), A10(7), A10(8), A10(9), A10(10), A10(11), \
                 "a120", "a121", "a122", "a123", "a124", "a125", "a126", "a127"
#define ACLOB_HI "a128", "a129", A10(13), A10(14), A10(15), A10(16), A10(17), A10(18), A10(19), A10(20), A10(21), A10(22), A10(23), A10(24), \
                 "a250", "a251", "a252", "a253", "a254", "a255"
#define ACLOB ACLOB_LO, ACLOB_HI

constexpr int SEQ = 2048, DM = 640, NH = 32, DH = 128, FF = 1728, FH = 864;
constexpr size_t WS_XN  = 0;
constexpr size_t WS_Z   = WS_XN  + (size_t)SEQ * DM * 2;
constexpr size_t WS_O   = WS_Z   + (size_t)SEQ * FF * 2;
constexpr size_t WS_Z2N = WS_O   + (size_t)SEQ * NH * DH * 2;
constexpr size_t WS_WSP = WS_Z2N + (size_t)SEQ * FH * 2;
constexpr size_t WS_SP  = WS_WSP + (size_t)SEQ * SEQ * 2;
constexpr size_t WS_U   = WS_SP  + (size_t)2 * SEQ * DM * 4;
constexpr size_t WS_END = WS_U   + (size_t)SEQ * FH * 2;

DI unsigned rfl(unsigned x) { return __builtin_amdgcn_readfirstlane(x); }
DI unsigned cvtpk(float lo, float hi) { f32x2 v = {lo, hi}; bf16x2 b = __builtin_convertvector(v, bf16x2); return __builtin_bit_cast(unsigned, b); }
DI float bflo(unsigned w) { return __uint_as_float(w << 16); }
DI float bfhi(unsigned w) { return __uint_as_float(w & 0xffff0000u); }
DI rsrc_t mk_srd(const void* p, unsigned bytes) { return __builtin_amdgcn_make_buffer_rsrc((void*)p, (short)0, (int)bytes, SRD_WORD3); }
DI float max3f(float a, float b, float c) { float r; asm("v_max3_f32 %0, %1, %2, %3" : "=v"(r) : "v"(a), "v"(b), "v"(c)); return r; }
DI float max2f(float a, float b) { float r; asm("v_max_f32_e32 %0, %1, %2" : "=v"(r) : "v"(a), "v"(b)); return r; }
DI float fadd_s(float a, float b) { float r; asm("v_add_f32_e32 %0, %1, %2" : "=v"(r) : "v"(a), "v"(b)); return r; }
DI float fmul_s(float a, float b) { float r; asm("v_mul_f32_e32 %0, %1, %2" : "=v"(r) : "v"(a), "v"(b)); return r; }
DI float fsub_s(float a, float b) { float r; asm("v_sub_f32_e32 %0, %1, %2" : "=v"(r) : "v"(a), "v"(b)); return r; }
template <int N> DI void wait_vm_bar() { asm volatile("s_waitcnt vmcnt(%c0) lgkmcnt(0)\n\ts_barrier" :: "i"(N > 63 ? 63 : N) : "memory"); }
DI void dma16(unsigned m0v, unsigned voff, rsrc_t srd, unsigned soff) {
  asm volatile("s_mov_b32 m0, %0\n\ts_nop 0\n\tbuffer_load_dwordx4 %1, %2, %3 offen lds" :: "s"(m0v), "v"(voff), "s"(srd), "s"(soff) : "m0", "memory");
}
DI void touch8k(const char* p) { unsigned d0, d1; asm volatile("global_load_dword %0, %2, off\n\tglobal_load_dword %1, %2, off offset:64" : "=&v"(d0), "=&v"(d1) : "v"(p) : "memory"); }
template <unsigned BYTES, int AREG> DI void code_touch(unsigned lane, unsigned wid) {
  unsigned long long pc; asm volatile("s_getpc_b64 %0" : "=s"(pc));
  const rsrc_t srd = mk_srd((const void*)pc, BYTES);
  const unsigned v = wid * 8192u + lane * 128u;
  asm volatile("s_nop 4\n\tbuffer_load_dword a[%c2], %0, %1, 0 offen\n\tbuffer_load_dword a[%c2], %0, %1, 0 offen offset:64" :: "v"(v), "s"(srd), "i"(AREG) : "memory", ACLOB);
  if constexpr (BYTES > 32768u) asm volatile("buffer_load_dword a[%c2], %0, %1, 0 offen\n\tbuffer_load_dword a[%c2], %0, %1, 0 offen offset:64" :: "v"(v + 32768u), "s"(srd), "i"(AREG) : "memory", ACLOB);
}
template <bool NATK, bool SWAPN> DI unsigned tr_lane(unsigned lane, unsigned halfstride) {
  const unsigned c = lane & 3u, cs = SWAPN ? (((c & 1u) << 1) | (c >> 1)) : c, hi = lane >> 5;
  return (cs << 3) | (((lane >> 2) & 3u) << 6) | (((lane >> 4) & 1u) << 5) | (NATK ? hi * halfstride : (hi << 8));
}

#define TS(k, i)
__global__ __launch_bounds__(256) void k_ln(const unsigned short* __restrict__ x, unsigned char* __restrict__ xnF, const unsigned short* __restrict__ w_in, const unsigned short* __restrict__ wq) {
  const unsigned lane = threadIdx.x & 63u, wid = threadIdx.x >> 6, l = lane & 31u;
  if (blockIdx.x >= 256u) {
    if (blockIdx.x >= 484u) {
      unsigned long long pc; asm volatile("s_getpc_b64 %0" : "=s"(pc)); unsigned d;
      asm volatile("s_nop 4\n\tbuffer_load_dword %0, %1, %2, 0 offen\n\tbuffer_load_dword %0, %1, %2, 0 offen offset:64" : "=&v"(d)
                   : "v"((((blockIdx.x - 484u) >> 3) * 4u + wid) * 8192u + lane * 128u), "s"(mk_srd((const void*)pc, 122880u)) : "memory");
      return;
    }
    const unsigned gw = (blockIdx.x - 256u) * 4u + wid;
    if (gw < 270u) touch8k((const char*)w_in + gw * 8192u + lane * 128u); else if (gw < 910u) touch8k((const char*)wq + (gw - 270u) * 8192u + lane * 128u);
    return;
  }
  const unsigned row = blockIdx.x * 8u + wid * 2u + (lane >> 5);
  const u32x4* src = (const u32x4*)(x + (size_t)row * DM);
  u32x4 v[3]; v[0] = src[l]; v[1] = src[l + 32]; v[2] = (l < 16u) ? src[l + 64] : (u32x4){0u, 0u, 0u, 0u};
  float f[24]; float s = 0.f;
#pragma unroll
  for (int i = 0; i < 3; ++i)
#pragma unroll
    for (int e = 0; e < 4; ++e) { f[8 * i + 2 * e] = bflo(v[i][e]); f[8 * i + 2 * e + 1] = bfhi(v[i][e]); s += f[8 * i + 2 * e] + f[8 * i + 2 * e + 1]; }
#pragma unroll
  for (int o = 16; o; o >>= 1) s += __shfl_xor(s, o);
  const float mean = s * (1.f / DM); float q = 0.f;
#pragma unroll
  for (int i = 0; i < 24; ++i) { f[i] -= mean; if (i < 16 || l < 16u) q += f[i] * f[i]; }
#pragma unroll
  for (int o = 16; o; o >>= 1) q += __shfl_xor(q, o);
  const float rstd = 1.f / sqrtf(q * (1.f / DM) + 1e-5f);
#pragma unroll
  for (int i = 0; i < 3; ++i) {
    const unsigned c = l + 32u * i;
    if (i < 2 || l < 16u) {
      u32x4 w;
#pragma unroll
      for (int e = 0; e < 4; ++e) w[e] = cvtpk(f[8 * i + 2 * e] * rstd, f[8 * i + 2 * e + 1] * rstd);
      *(u32x4*)(xnF + ((size_t)((c >> 1) * 64u + (row >> 5)) * 64u + (c & 1u) * 32u + (row & 31u)) * 16u) = w;
    }
  }
}

constexpr int LA = 6;
template <int NT, int KS> struct GemmC {
  static constexpr int P = NT * KS / 4, TILEB = NT * KS * 1024, G = P + KS, NM = NT * KS;
  unsigned vw[P];
  unsigned vx[KS];
  unsigned vtr;
  unsigned m0w;
  unsigned wstride;
  rsrc_t srdW, srdX;
  int kid;
  f32x16 acc[NT];
};
struct NoEx { static constexpr int N = 0; template <int J> DI void issue() {} };
template <int KS> constexpr int GX(int u, int ks) { return 32 + (u * KS + ks) * 4; }
template <int NT, int KS, bool NATK, int AS, int F> DI void g_tr(unsigned vaddr) {
  constexpr int ks = F / NT, nt = F % NT, a = 4 * AS;
  constexpr int o0 = NATK ? ((2 * ks) * NT + nt) * 512 : ((2 * ks) * NT + nt) * 512, o1 = NATK ? o0 + 256 : o0 + NT * 512;
  asm volatile("ds_read_b64_tr_b16 a[%c1:%c2], %0 offset:%c5\n\tds_read_b64_tr_b16 a[%c3:%c4], %0 offset:%c6" :: "v"(vaddr), "i"(a), "i"(a + 1), "i"(a + 2), "i"(a + 3), "i"(o0), "i"(o1) : "memory", ACLOB);
}
template <int NT, int KS, int XI, int KSI> DI void g_xload(GemmC<NT, KS>& c, unsigned soff) {
  constexpr int a = GX<KS>(XI, KSI);
  asm volatile("buffer_load_dwordx4 a[%c3:%c4], %0, %1, %2 offen" :: "v"(c.vx[KSI]), "s"(c.srdX), "s"(soff), "i"(a), "i"(a + 3) : "memory", ACLOB);
}
template <int NT, int KS, int AS, int XI, bool ZERO, int LG, int M> DI void g_mfma(GemmC<NT, KS>& c) {
  constexpr int ks = M / NT, nt = M % NT, a = 4 * AS, b = GX<KS>(XI, ks);
  if constexpr (ZERO && ks == 0) asm volatile("s_waitcnt lgkmcnt(%c5)\n\tv_mfma_f32_32x32x16_bf16 %0, a[%c1:%c2], a[%c3:%c4], 0" : "=v"(c.acc[nt]) : "i"(a), "i"(a + 3), "i"(b), "i"(b + 3), "i"(LG) : "memory", ACLOB);
  else asm volatile("s_waitcnt lgkmcnt(%c5)\n\tv_mfma_f32_32x32x16_bf16 %0, a[%c1:%c2], a[%c3:%c4], %0" : "+v"(c.acc[nt]) : "i"(a), "i"(a + 3), "i"(b), "i"(b + 3), "i"(LG) : "memory", ACLOB);
}
template <int NT, int KS, int D, int GS, int Q> DI void g_vmem(GemmC<NT, KS>& c, unsigned g) {
  constexpr int P = GemmC<NT, KS>::P, G = GemmC<NT, KS>::G, gi = Q / G, q = Q % G, slot = (GS + gi) % D;
  if constexpr (q < P) dma16(c.m0w + slot * GemmC<NT, KS>::TILEB + 1024u * q, c.vw[q], c.srdW, (g + gi) * c.wstride);
  else g_xload<NT, KS, slot, q - P>(c, (g + gi) * (KS * 65536u));
}
template <int NT, int KS, int D, int... Q> DI void g_pre(GemmC<NT, KS>& c, iseq<Q...>) { (g_vmem<NT, KS, D, 0, Q>(c, 0u), ...); }
template <int NT, int KS, bool NATK, int D, int TM, bool FIRST, bool LAST, int GS, int NG, bool EXS, class EX, int J>
DI void g_gap(GemmC<NT, KS>& c, EX& ex, unsigned vrd, unsigned g) {
  constexpr int NM = NT * KS, NOP = NG * GemmC<NT, KS>::G, OPG = (NOP + NM - 1) / NM, T1 = (TM + 1) % D;
  if constexpr (J < LA) { if constexpr (!FIRST) g_mfma<NT, KS, (TM * NM + NM - LA + J) & 7, TM, false, (LAST ? 2 * (LA - 1 - J) : 2 * (LA - 1)), NM - LA + J>(c); }
  else if constexpr (!LAST) g_mfma<NT, KS, (T1 * NM + J - LA) & 7, T1, FIRST, 2 * (LA - 1), J - LA>(c);
  if constexpr (!LAST) g_tr<NT, KS, NATK, (T1 * NM + J) & 7, J>(vrd);
  if constexpr (OPG * J < NOP) g_vmem<NT, KS, D, GS, OPG * J>(c, g);
  if constexpr (OPG == 2 && 2 * J + 1 < NOP) g_vmem<NT, KS, D, GS, 2 * J + 1>(c, g);
  if constexpr (EXS) { if constexpr (2 * J < EX::N) ex.template issue<2 * J>(); if constexpr (2 * J + 1 < EX::N) ex.template issue<2 * J + 1>(); }
  GAP();
}
template <int NT, int KS, bool NATK, int D, int TM, bool FIRST, bool LAST, int GS, int NG, bool EXS, int VM, class EX, int... Js>
DI void g_step_(GemmC<NT, KS>& c, EX& ex, unsigned g, iseq<Js...>) {
  const unsigned vrd = c.vtr + ((TM + 1) % D) * GemmC<NT, KS>::TILEB;
  if constexpr (!LAST) asm volatile("s_waitcnt vmcnt(%c0)\n\ts_barrier" :: "i"(VM > 63 ? 63 : VM) : "memory");
  if constexpr (FIRST) TS(c.kid, 1);
  GAP();
  (g_gap<NT, KS, NATK, D, TM, FIRST, LAST, GS, NG, EXS, EX, Js>(c, ex, vrd, g), ...);
}
constexpr int g_iss(int t, int NTILES, int PRE) {
  int i = 1;
  for (int u = -1; u <= t; ++u) { i += 2; if (i > u + 1 + PRE) i = u + 1 + PRE; if (i > NTILES - 1) i = NTILES - 1; }
  return i;
}
template <int NT, int KS, bool NATK, int NTILES, int D, int PRE, class EX, int T> DI void g_step_static(GemmC<NT, KS>& c, EX& ex) {
  constexpr int G = GemmC<NT, KS>::G, i0 = g_iss(T - 1, NTILES, PRE), i1 = g_iss(T, NTILES, PRE);
  constexpr bool done = (i0 == NTILES - 1), done_before = (g_iss(T - 2, NTILES, PRE) == NTILES - 1);
  constexpr int VM = (i0 - (T + 1)) * G + (done_before ? EX::N : 0);
  g_step_<NT, KS, NATK, D, (T + D) % D, (T == -1), (T == NTILES - 1), (i0 + 1) % D, i1 - i0, (done && !done_before), VM, EX>(c, ex, i0 + 1, make_iseq<(T == NTILES - 1) ? LA : NT * KS>{});
}
template <int NT, int KS, bool NATK, int NTILES, int D, int PRE, class EX, int T0, int... Ts> DI void g_tail(GemmC<NT, KS>& c, EX& ex, iseq<Ts...>) {
  (g_step_static<NT, KS, NATK, NTILES, D, PRE, EX, T0 + Ts>(c, ex), ...);
}
template <int NT, int KS, bool NATK, int D, int PRE, class EX, int... Ts> DI void g_body(GemmC<NT, KS>& c, EX& ex, int t, iseq<Ts...>) {
  (g_step_<NT, KS, NATK, D, Ts, false, false, (Ts + 1 + PRE) % D, 1, false, (PRE - 1) * GemmC<NT, KS>::G, EX>(c, ex, t + Ts + 1 + PRE, make_iseq<NT * KS>{}), ...);
}
template <int NT, int KS, bool NATK, int NTILES, int D, int PRE, class EX> DI void gemm_run(GemmC<NT, KS>& c, EX& ex) {
  static_assert(NTILES <= D || (PRE <= D - 2 && (D * NT * KS) % 8 == 0), "ring");
  static_assert(32 + D * KS * 4 <= 256 && 2 * NT * KS >= EX::N, "regs");
  TS(c.kid, 0);
  asm volatile("s_nop 4" ::: "memory");
  g_pre<NT, KS, D>(c, make_iseq<2 * GemmC<NT, KS>::G>{});
  if constexpr (NTILES <= D) g_tail<NT, KS, NATK, NTILES, D, PRE, EX, -1>(c, ex, make_iseq<NTILES + 1>{});
  else {
    constexpr int T0 = D * ((NTILES - 1 - PRE) / D);
    g_tail<NT, KS, NATK, NTILES, D, PRE, EX, -1>(c, ex, make_iseq<D + 1>{});
    for (int t = D; t < T0; t += D) g_body<NT, KS, NATK, D, PRE, EX>(c, ex, t, make_iseq<D>{});
    g_tail<NT, KS, NATK, NTILES, D, PRE, EX, T0>(c, ex, make_iseq<NTILES - T0>{});
  }
  asm volatile("s_waitcnt vmcnt(0)\n\ts_nop 7\n\ts_nop 7" ::: "memory");
#pragma unroll
  for (int nt = 0; nt < NT; ++nt) asm volatile("" : "+v"(c.acc[nt]));
  TS(c.kid, 2);
  GAP();
}
template <int NT, int KS> DI void set_vx(GemmC<NT, KS>& c, unsigned v) {
#pragma unroll
  for (int k = 0; k < KS; ++k) c.vx[k] = v + k * 65536u;
}
DI void bload16(u32x4& r, unsigned voff, rsrc_t srd, unsigned soff) { asm volatile("buffer_load_dwordx4 %0, %1, %2, %3 offen" : "=v"(r) : "v"(voff), "s"(srd), "s"(soff) : "memory"); }
template <int NT, int KS, bool NATK, bool SWAPN> DI void gemm_setup_rm(GemmC<NT, KS>& c, unsigned ldsb, unsigned lane, unsigned wid, unsigned ldB) {
  constexpr int P = GemmC<NT, KS>::P;
#pragma unroll
  for (int q = 0; q < P; ++q) { const unsigned p = wid * P + q, kg = p / (NT / 2), cp = p % (NT / 2);
    c.vw[q] = ldB * (8u * kg + ((lane >> 2) & 7u)) + 128u * cp + 64u * (lane >> 5) + 16u * (lane & 3u); }
  c.wstride = ldB * 16u * KS;
  c.m0w = rfl(ldsb + 1024u * P * wid);
  c.vtr = ldsb + tr_lane<NATK, SWAPN>(lane, NT * 512u);
}

DI float gelu_tanh(float v) {
  const float y2 = v * (-2.3022081985f - 0.1029432397f * v * v);
  return v * __builtin_amdgcn_rcpf(1.f + __builtin_amdgcn_exp2f(y2));
}
__global__ __launch_bounds__(256, 1) void k_zgemm(const unsigned char* __restrict__ xnF, const unsigned short* __restrict__ w_in, unsigned char* __restrict__ zF) {
  extern __shared__ __attribute__((aligned(16))) char lds[];
  const unsigned lane = threadIdx.x & 63u, wid = rfl(threadIdx.x >> 6);
  const unsigned xcd = blockIdx.x & 7u, idx = blockIdx.x >> 3;
  const unsigned rb128 = 4u * (xcd >> 1) + idx / 7u, cb = 7u * (xcd & 1u) + idx % 7u, rb = 4u * rb128 + wid;
  GemmC<4, 5> c; c.kid = 1;
  gemm_setup_rm<4, 5, true, false>(c, (unsigned)(size_t)lds, lane, wid, FF * 2u);
  c.srdW = mk_srd((const char*)w_in + cb * 256u, DM * FF * 2u - cb * 256u);
  c.srdX = mk_srd(xnF, SEQ * DM * 2u);
  set_vx(c, rb * 1024u + lane * 16u);
  NoEx ex; gemm_run<4, 5, true, 8, 8, 6>(c, ex);
#pragma unroll
  for (int nt = 0; nt < 4; ++nt) {
    const unsigned ct = 4u * cb + nt;
    if (ct < 54u) {
#pragma unroll
      for (int a = 0; a < 2; ++a) {
        u32x4 w;
#pragma unroll
        for (int e = 0; e < 4; ++e) w[e] = cvtpk(gelu_tanh(c.acc[nt][8 * a + 2 * e]), gelu_tanh(c.acc[nt][8 * a + 2 * e + 1]));
        *(u32x4*)(zF + ((size_t)((2u * ct + a) * 64u + rb) * 64u + lane) * 16u) = w;
      }
    }
  }
}

template <int NT> struct ExZ { static constexpr int N = 2 * NT; u32x4 r[N]; unsigned voff; rsrc_t srd; template <int J> DI void issue() { bload16(r[J], voff, srd, J * 65536u); } };
template <int NT> DI void spatial_unit(char* lds, unsigned lane, unsigned wid, unsigned rb, unsigned ct0, const unsigned char* wspF, const unsigned char* z2nT,
                                       const unsigned char* zF, const unsigned short* b_sp, unsigned char* uF) {
  GemmC<NT, 4> c; c.kid = 3;
  const unsigned ldsb = (unsigned)(size_t)lds;
#pragma unroll
  for (int q = 0; q < NT; ++q) { const unsigned sg = 2u * (wid * NT + q) + (lane >> 5), kg = sg / NT, nt = sg % NT;
    c.vw[q] = (kg * 27u + ct0 + nt) * 512u + (lane & 31u) * 16u; }
  c.wstride = 8u * 27u * 512u;
  c.m0w = rfl(ldsb + 1024u * NT * wid);
  c.vtr = ldsb + tr_lane<true, true>(lane, NT * 512u);
  c.srdW = mk_srd(z2nT, SEQ * FH * 2u);
  c.srdX = mk_srd(wspF, SEQ * SEQ * 2u);
  set_vx(c, rb * 1024u + lane * 16u);
  const float bias = bflo((unsigned)b_sp[rb * 32u + (lane & 31u)]);
  ExZ<NT> ex; ex.voff = (2u * ct0 * 64u + rb) * 1024u + lane * 16u; ex.srd = mk_srd(zF, SEQ * FF * 2u);
  asm volatile("s_waitcnt vmcnt(0)" :: "v"(bias) : "memory");
  gemm_run<NT, 4, true, 32, 8, 6>(c, ex);
#pragma unroll
  for (int j = 0; j < 2 * NT; ++j) asm volatile("" : "+v"(ex.r[j]));
#pragma unroll
  for (int nt = 0; nt < NT; ++nt)
#pragma unroll
    for (int a = 0; a < 2; ++a) {
      const unsigned ks = 2u * (ct0 + nt) + a;
      const u32x4 z1 = ex.r[2 * nt + a];
      u32x4 w;
#pragma unroll
      for (int e = 0; e < 4; ++e) w[e] = cvtpk(bflo(z1[e]) * (c.acc[nt][8 * a + 2 * e] + bias), bfhi(z1[e]) * (c.acc[nt][8 * a + 2 * e + 1] + bias));
      *(u32x4*)(uF + ((size_t)(ks * 64u + rb) * 64u + lane) * 16u) = w;
    }
}
__global__ __launch_bounds__(256, 1) void k_mid(const unsigned char* __restrict__ OF, const unsigned short* __restrict__ wo, unsigned char* __restrict__ sP,
                                                const unsigned char* __restrict__ wspF, const unsigned char* __restrict__ z2nT, const unsigned char* __restrict__ zF,
                                                const unsigned short* __restrict__ b_sp, unsigned char* __restrict__ uF) {
  extern __shared__ __attribute__((aligned(16))) char lds[];
  const unsigned lane = threadIdx.x & 63u, wid = rfl(threadIdx.x >> 6);
  const unsigned xcd = blockIdx.x & 7u, idx = blockIdx.x >> 3;
  if (idx >= 12u) {
    const unsigned i = idx - 12u, kh = xcd & 1u, rb128 = 4u * (xcd >> 1) + i / 5u, cb = i % 5u, rb = 4u * rb128 + wid;
    GemmC<4, 4> c; c.kid = 3;
    gemm_setup_rm<4, 4, false, true>(c, (unsigned)(size_t)lds, lane, wid, DM * 2u);
    const unsigned wofs = kh * 2048u * DM * 2u + cb * 256u;
    c.srdW = mk_srd((const char*)wo + wofs, NH * DH * DM * 2u - wofs);
    c.srdX = mk_srd(OF, SEQ * NH * DH * 2u);
    set_vx(c, (kh * 128u * 64u + rb) * 1024u + lane * 16u);
    NoEx ex; gemm_run<4, 4, false, 32, 8, 6>(c, ex);
#pragma unroll
    for (int nt = 0; nt < 4; ++nt)
#pragma unroll
      for (int a = 0; a < 2; ++a) {
        u32x4 w;
#pragma unroll
        for (int e = 0; e < 4; ++e) w[e] = cvtpk(c.acc[nt][8 * a + 2 * e], c.acc[nt][8 * a + 2 * e + 1]);
        *(u32x4*)(sP + ((size_t)(((kh * 64u + rb) * 20u + 4u * cb + nt) * 2u + a) * 64u + lane) * 16u) = w;
      }
  } else {
    const unsigned rb128 = 4u * (xcd >> 1) + idx / 3u, j = 3u * (xcd & 1u) + idx % 3u, rb = 4u * rb128 + wid;
    if (j & 1u) spatial_unit<4>(lds, lane, wid, rb, 15u + 4u * (j >> 1), wspF, z2nT, zF, b_sp, uF);
    else        spatial_unit<5>(lds, lane, wid, rb, 5u * (j >> 1), wspF, z2nT, zF, b_sp, uF);
  }
}

struct ExO { static constexpr int N = 12; u32x4 r[N]; unsigned vs, vxx; rsrc_t srdS, srdXx;
  template <int J> DI void issue() { if constexpr (J < 8) bload16(r[J], vs, srdS, (J >> 2) * (64u * 20u * 2048u) + (J & 3) * 1024u); else bload16(r[J], vxx, srdXx, (J - 8) * 32u); } };
__global__ __launch_bounds__(256, 1) void k_out(const unsigned char* __restrict__ uF, const unsigned short* __restrict__ w_out, const unsigned char* __restrict__ sP,
                                                const unsigned short* __restrict__ x, unsigned short* __restrict__ out) {
  extern __shared__ __attribute__((aligned(16))) char lds[];
  const unsigned lane = threadIdx.x & 63u, wid = rfl(threadIdx.x >> 6);
  const unsigned xcd = blockIdx.x & 7u, idx = blockIdx.x >> 3;
  const unsigned rb128 = 4u * (xcd >> 1) + idx / 5u, cb = 5u * (xcd & 1u) + idx % 5u, rb = 4u * rb128 + wid;
  GemmC<2, 6> c; c.kid = 4;
  gemm_setup_rm<2, 6, false, true>(c, (unsigned)(size_t)lds, lane, wid, DM * 2u);
  c.srdW = mk_srd((const char*)w_out + cb * 128u, FH * DM * 2u - cb * 128u);
  c.srdX = mk_srd(uF, SEQ * FH * 2u);
  set_vx(c, rb * 1024u + lane * 16u);
  const unsigned row = rb * 32u + (lane & 31u), hi = lane >> 5;
  ExO ex; ex.vs = (rb * 20u + 2u * cb) * 2048u + lane * 16u; ex.vxx = row * 1280u + cb * 128u + hi * 16u;
  ex.srdS = mk_srd(sP, 2u * SEQ * DM * 2u); ex.srdXx = mk_srd(x, SEQ * DM * 2u);
  gemm_run<2, 6, false, 9, 9, 6>(c, ex);
#pragma unroll
  for (int j = 0; j < 12; ++j) asm volatile("" : "+v"(ex.r[j]));
#pragma unroll
  for (int nt = 0; nt < 2; ++nt) {
    const unsigned ct = 2u * cb + nt;
#pragma unroll
    for (int a = 0; a < 2; ++a) {
      const size_t eo = (size_t)row * DM + 32u * ct + 16u * a + 8u * hi;
      const u32x4 xv = ex.r[8 + 2 * nt + a];
      float sv[8];
#pragma unroll
      for (int e = 0; e < 4; ++e) { const unsigned p0 = ex.r[2 * nt + a][e], p1 = ex.r[4 + 2 * nt + a][e]; sv[2 * e] = bflo(p0) + bflo(p1); sv[2 * e + 1] = bfhi(p0) + bfhi(p1); }
      u32x4 w;
#pragma unroll
      for (int e = 0; e < 4; ++e) {
        const float b0 = c.acc[nt][8 * a + 2 * e], b1 = c.acc[nt][8 * a + 2 * e + 1];
        const float g0 = b0 * __builtin_amdgcn_rcpf(1.f + __builtin_amdgcn_exp2f(-1.4426950408889634f * b0));
        const float g1 = b1 * __builtin_amdgcn_rcpf(1.f + __builtin_amdgcn_exp2f(-1.4426950408889634f * b1));
        w[e] = cvtpk(bflo(xv[e]) + sv[2 * e] * g0, bfhi(xv[e]) + sv[2 * e + 1] * g1);
      }
      *(u32x4*)(out + eo) = w;
    }
  }
}

constexpr int NTILE = SEQ / 64;
constexpr unsigned KRS = 256u, KTILEB = 64u * KRS;
constexpr float THR = 8.0f;
#define KRING 0x0000u
#define VRING 0x10000u
#define WRING 0x0u
DI float w4_aone() { unsigned l; asm volatile("v_mbcnt_lo_u32_b32 %0, -1, 0\n\tv_mbcnt_hi_u32_b32 %0, -1, %0" : "=v"(l)); return l < 32u ? 1.f : 0.f; }
DI void negm_bcast(f32x16& negm, float aone, float neg_mhat) { asm volatile("s_nop 1\n\tv_mfma_f32_32x32x2_f32 %0, %1, %2, 0" : "+v"(negm) : "v"(aone), "v"(neg_mhat)); }
struct W4C {
  unsigned vkb[4], vvb, vksrc[2], vvsrc, m0k, m0v, sofk, sofv, ldsb;
  rsrc_t srdK, srdV;
  f32x16 S[2][2][2];
  f32x16 NEGM[2];
  bf16x8 Vf[4][4];
  bf16x8 Pk[2][4];
  bf16x4 vlo[4][4], vup[4][4];
  float mhat[2], lsum[2], fct[2]; bool pend;
  float rs0[2], rs1[2], pmx0[2], pmx1[2], rmax[2], delta[2];
  unsigned pm0[8], psf[8];
  u32x4 sw[2]; unsigned vsl, vss, ssl, sss; rsrc_t srdSL, srdSS;
};
constexpr int AO_(int db, int qb) { return 16 * (2 * db + qb); }
constexpr int AQ_(int qb, int d0) { return 128 + 32 * qb + 4 * d0; }
constexpr int AK_(int kb, int d0) { return 192 + 32 * kb + 4 * d0; }
template <int I, int SO = 0> DI void tr_read(W4C& c, unsigned vslot) {
  constexpr int ks = I >> 3, db = (I >> 1) & 3;
  if constexpr ((I & 1) == 0) asm volatile("ds_read_b64_tr_b16 %0, %1 offset:%2" : "=v"(c.vlo[db][ks]) : "v"(c.vvb + vslot), "i"(SO + 4096 * ks + 512 * db) : "memory");
  else                        asm volatile("ds_read_b64_tr_b16 %0, %1 offset:%2" : "=v"(c.vup[db][ks]) : "v"(c.vvb + vslot), "i"(SO + 4096 * ks + 2048 + 512 * db) : "memory");
}
template <int I, int SO = 0> DI void k_read(W4C& c, unsigned slot) {
  constexpr int kb = I >> 3, d0 = I & 7;
  asm volatile("ds_read_b128 a[%c1:%c2], %0 offset:%c3" :: "v"(c.vkb[d0 & 3] + slot), "i"(AK_(kb, d0)), "i"(AK_(kb, d0) + 3), "i"(SO + 8192 * kb + 128 * (d0 >> 2)) : "memory", ACLOB_HI);
}
template <int X, bool FIRST, int M> DI void qk_mfma(W4C& c) {
  constexpr int d0 = M >> 2, kb = (M >> 1) & 1, qb = M & 1, ak = AK_(kb, d0), aq = AQ_(qb, d0);
  if constexpr (d0 == 0) {
    if constexpr (FIRST) asm volatile("v_mfma_f32_32x32x16_bf16 %0, a[%c1:%c2], a[%c3:%c4], 0" : "=&v"(c.S[X][kb][qb]) : "i"(ak), "i"(ak + 3), "i"(aq), "i"(aq + 3) : ACLOB);
    else                 asm volatile("v_mfma_f32_32x32x16_bf16 %0, a[%c2:%c3], a[%c4:%c5], %1" : "=&v"(c.S[X][kb][qb]) : "v"(c.NEGM[qb]), "i"(ak), "i"(ak + 3), "i"(aq), "i"(aq + 3) : ACLOB);
  } else asm volatile("v_mfma_f32_32x32x16_bf16 %0, a[%c1:%c2], a[%c3:%c4], %0" : "+v"(c.S[X][kb][qb]) : "i"(ak), "i"(ak + 3), "i"(aq), "i"(aq + 3) : ACLOB);
}
template <int M, bool ZERO> DI void pv_mfma(W4C& c) {
  constexpr int ks = M >> 3, db = (M >> 1) & 3, qb = M & 1, ao = AO_(db, qb);
  if constexpr (ZERO && ks == 0) asm volatile("v_mfma_f32_32x32x16_bf16 a[%c2:%c3], %0, %1, 0" :: "v"(c.Vf[db][ks]), "v"(c.Pk[qb][ks]), "i"(ao), "i"(ao + 15) : ACLOB_LO);
  else                         asm volatile("v_mfma_f32_32x32x16_bf16 a[%c2:%c3], %0, %1, a[%c2:%c3]" :: "v"(c.Vf[db][ks]), "v"(c.Pk[qb][ks]), "i"(ao), "i"(ao + 15) : ACLOB_LO);
}
template <int X, int QB, int KS, int P> DI void pack1(W4C& c) {
  u32x4 w = __builtin_bit_cast(u32x4, c.Pk[QB][KS]);
  w[P] = cvtpk(c.S[X][KS >> 1][QB][8 * (KS & 1) + 2 * P], c.S[X][KS >> 1][QB][8 * (KS & 1) + 2 * P + 1]);
  c.Pk[QB][KS] = __builtin_bit_cast(bf16x8, w);
}
template <int X, int J> DI void rowsum1(W4C& c) {
  constexpr int qb = J >> 4, ks = (J >> 2) & 3, p = J & 3;
  c.rs0[qb] = fadd_s(c.rs0[qb], c.S[X][ks >> 1][qb][8 * (ks & 1) + 2 * p]); c.rs1[qb] = fadd_s(c.rs1[qb], c.S[X][ks >> 1][qb][8 * (ks & 1) + 2 * p + 1]);
}
template <int X, bool FIRST, int M> DI void gapA(W4C& c, unsigned vslot, const int t) {
  constexpr int Xp = X ^ 1;
  qk_mfma<X, FIRST, M>(c);
  if constexpr (M >= 16 && M <= 31) {
    constexpr int q = (M - 16) >> 1;
    if constexpr (((M - 16) & 1) == 0) {
      if constexpr (q < 4) c.pm0[q] = c.m0k + (unsigned)((t + 2) & 1) * 0x4000u + 1024u * q; else c.pm0[q] = c.m0v + (unsigned)((t + 1) & 1) * 0x4000u + 1024u * (q - 4);
      asm volatile("" : "+s"(c.pm0[q]));
    } else {
      if constexpr (q < 4) c.psf[q] = KTILEB * (unsigned)(t + 2) + c.sofk + KRS * 4u * q; else c.psf[q] = KTILEB * (unsigned)(t + 1) + c.sofv + KRS * 8u * ((q - 4) >> 1) + 128u * ((q - 4) & 1);
      asm volatile("" : "+s"(c.psf[q]));
    }
  }
  if constexpr (M == 31) asm volatile("s_nop 4" : "+s"(c.pm0[0]), "+s"(c.pm0[1]), "+s"(c.pm0[2]), "+s"(c.pm0[3]), "+s"(c.pm0[4]), "+s"(c.pm0[5]), "+s"(c.pm0[6]), "+s"(c.pm0[7]),
                                                  "+s"(c.psf[0]), "+s"(c.psf[1]), "+s"(c.psf[2]), "+s"(c.psf[3]), "+s"(c.psf[4]), "+s"(c.psf[5]), "+s"(c.psf[6]), "+s"(c.psf[7]));
  if constexpr (!FIRST && M == 8) asm volatile("" :: "v"(c.NEGM[0]), "v"(c.NEGM[1]));
  if constexpr (!FIRST) {
    constexpr int base = M + (M >= 4) + (M >= 8) + (M >= 12) + (M >= 16) + (M >= 20) + (M >= 24);
    if constexpr (M <= 25) tr_read<base>(c, vslot);
    if constexpr (M == 3 || M == 7 || M == 11 || M == 15 || M == 19 || M == 23) tr_read<base + 1>(c, vslot);
    if constexpr (M < 16) {
      constexpr int e0 = 2 * M, e1 = 2 * M + 1;
      c.S[Xp][1][e0 >> 4][e0 & 15] = __builtin_amdgcn_exp2f(c.S[Xp][1][e0 >> 4][e0 & 15]);
      c.S[Xp][1][e1 >> 4][e1 & 15] = __builtin_amdgcn_exp2f(c.S[Xp][1][e1 >> 4][e1 & 15]);
      pack1<Xp, (M >> 3), ((M >> 2) & 1), (M & 3)>(c);
    } else {
      constexpr int cc = M - 16;
      pack1<Xp, (cc >> 3), 2 + ((cc >> 2) & 1), (cc & 3)>(c);
      rowsum1<Xp, 16 * (cc >> 3) + 8 + 4 * ((cc >> 2) & 1) + (cc & 3)>(c);
    }
  }
  GAP();
}
#define W4_VLO "+v"(c.vlo[0][0]), "+v"(c.vlo[0][1]), "+v"(c.vlo[0][2]), "+v"(c.vlo[0][3]), "+v"(c.vlo[1][0]), "+v"(c.vlo[1][1]), "+v"(c.vlo[1][2]), "+v"(c.vlo[1][3]), \
               "+v"(c.vlo[2][0]), "+v"(c.vlo[2][1]), "+v"(c.vlo[2][2]), "+v"(c.vlo[2][3]), "+v"(c.vlo[3][0]), "+v"(c.vlo[3][1]), "+v"(c.vlo[3][2]), "+v"(c.vlo[3][3])
#define W4_VUP "+v"(c.vup[0][0]), "+v"(c.vup[0][1]), "+v"(c.vup[0][2]), "+v"(c.vup[0][3]), "+v"(c.vup[1][0]), "+v"(c.vup[1][1]), "+v"(c.vup[1][2]), "+v"(c.vup[1][3]), \
               "+v"(c.vup[2][0]), "+v"(c.vup[2][1]), "+v"(c.vup[2][2]), "+v"(c.vup[2][3]), "+v"(c.vup[3][0]), "+v"(c.vup[3][1]), "+v"(c.vup[3][2]), "+v"(c.vup[3][3])
DI void v_concat(W4C& c) {
#pragma unroll
  for (int ks = 0; ks < 4; ++ks)
#pragma unroll
    for (int db = 0; db < 4; ++db) c.Vf[db][ks] = __builtin_shufflevector(c.vlo[db][ks], c.vup[db][ks], 0, 1, 2, 3, 4, 5, 6, 7);
}
DI void lsum_merge(W4C& c) {
#pragma unroll
  for (int qb = 0; qb < 2; ++qb) {
    const float rs = fadd_s(c.rs0[qb], c.rs1[qb]);
    auto pr = __builtin_amdgcn_permlane32_swap(__float_as_uint(rs), __float_as_uint(rs), false, false);
    c.lsum[qb] = fadd_s(c.lsum[qb], fadd_s(__uint_as_float(pr[0]), __uint_as_float(pr[1])));
  }
}
template <int X, bool FIRST, int VMA, int... Ms> DI void phaseA_(W4C& c, const int t, iseq<Ms...>) {
  const unsigned vslot = (unsigned)((t - 1) & 1) * 0x4000u;
  (gapA<X, FIRST, Ms>(c, vslot, t), ...);
  if constexpr (!FIRST) lsum_merge(c);
  if constexpr (FIRST) asm volatile("s_waitcnt vmcnt(0) lgkmcnt(0)\n\ts_barrier" ::: "memory");
  else { asm volatile("s_waitcnt vmcnt(%c16) lgkmcnt(0)" : W4_VLO : "i"(VMA) : "memory"); asm volatile("s_barrier" : W4_VUP :: "memory"); }
  GAP();
  if constexpr (!FIRST) v_concat(c);
}
template <int X, bool FIRST, int VMA = 0> DI void phaseA(W4C& c, const int t) { phaseA_<X, FIRST, VMA>(c, t, make_iseq<32>{}); }

template <int X, bool PEEL, bool ZERO1, bool DK, bool DV, bool KR, int SJ, int M> DI void gapB(W4C& c, const unsigned kslot_rd) {
  if constexpr (!PEEL) pv_mfma<M, ZERO1>(c);
  if constexpr (M < 4 && DK) dma16(c.pm0[M], c.vksrc[M & 1], c.srdK, c.psf[M]);
  else if constexpr (M >= 4 && M < 8 && DV) dma16(c.pm0[M], c.vvsrc, c.srdV, c.psf[M]);
  if constexpr (M >= 1 && M <= 16 && KR) k_read<M - 1>(c, kslot_rd);
  if constexpr (M >= 8 && M < 16) {
    constexpr int i = M - 8, q = i >> 2, r = 4 * (i & 3);
    if constexpr ((i & 3) == 0) { c.pmx0[q] = max3f(c.S[X][0][q][0], c.S[X][0][q][1], c.S[X][1][q][0]); c.pmx1[q] = max3f(c.S[X][0][q][2], c.S[X][0][q][3], c.S[X][1][q][1]);
                                  c.pmx0[q] = max3f(c.pmx0[q], c.S[X][1][q][2], c.S[X][1][q][3]); }
    else                        { c.pmx0[q] = max3f(c.pmx0[q], c.S[X][0][q][r], c.S[X][0][q][r + 1]); c.pmx1[q] = max3f(c.pmx1[q], c.S[X][0][q][r + 2], c.S[X][0][q][r + 3]);
                                  c.pmx0[q] = max3f(c.pmx0[q], c.S[X][1][q][r], c.S[X][1][q][r + 1]); c.pmx1[q] = max3f(c.pmx1[q], c.S[X][1][q][r + 2], c.S[X][1][q][r + 3]); }
  }
  if constexpr (M == 16 || M == 17) {
    constexpr int q = M - 16; const float mm = max2f(c.pmx0[q], c.pmx1[q]);
    auto pr = __builtin_amdgcn_permlane32_swap(__float_as_uint(mm), __float_as_uint(mm), false, false);
    c.rmax[q] = max2f(__uint_as_float(pr[0]), __uint_as_float(pr[1]));
  }
  if constexpr (M == 18) {
    if constexpr (PEEL) {
#pragma unroll
      for (int q = 0; q < 2; ++q) { c.mhat[q] = c.rmax[q]; c.lsum[q] = 0.f; c.fct[q] = 1.f;
#pragma unroll
        for (int kb = 0; kb < 2; ++kb) { _Pragma("unroll") for (int e = 0; e < 16; ++e) c.S[X][kb][q][e] = fsub_s(c.S[X][kb][q][e], c.rmax[q]); }
        negm_bcast(c.NEGM[q], w4_aone(), -c.mhat[q]);
      }
    } else {
      const bool grew = (c.rmax[0] > THR) | (c.rmax[1] > THR);
      if (__builtin_expect(__any((int)grew), 0)) {
#pragma unroll
        for (int q = 0; q < 2; ++q) { const float dl = max2f(c.rmax[q], 0.f); c.delta[q] = dl; c.mhat[q] = fadd_s(c.mhat[q], dl); c.fct[q] = __builtin_amdgcn_exp2f(-dl);
#pragma unroll
          for (int kb = 0; kb < 2; ++kb) { _Pragma("unroll") for (int e = 0; e < 16; ++e) c.S[X][kb][q][e] = fsub_s(c.S[X][kb][q][e], dl); }
          negm_bcast(c.NEGM[q], w4_aone(), -c.mhat[q]);
        }
        c.pend = true;
      }
    }
  }
  if constexpr (M == 18) { c.rs0[0] = c.rs0[1] = c.rs1[0] = c.rs1[1] = 0.f; }
  if constexpr (M >= 19) {
    constexpr int g = M - 19, e_lo = (32 * g) / 13, e_hi = (32 * (g + 1)) / 13;
    {
      constexpr int p_lo = g ? (32 * (g - 1)) / 13 : 0, p_hi = g ? (32 * g) / 13 : 0;
      if constexpr (p_lo + 0 < p_hi) { constexpr int e = p_lo + 0; if constexpr (e & 1) c.rs1[e >> 4] = fadd_s(c.rs1[e >> 4], c.S[X][0][e >> 4][e & 15]); else c.rs0[e >> 4] = fadd_s(c.rs0[e >> 4], c.S[X][0][e >> 4][e & 15]); }
      if constexpr (p_lo + 1 < p_hi) { constexpr int e = p_lo + 1; if constexpr (e & 1) c.rs1[e >> 4] = fadd_s(c.rs1[e >> 4], c.S[X][0][e >> 4][e & 15]); else c.rs0[e >> 4] = fadd_s(c.rs0[e >> 4], c.S[X][0][e >> 4][e & 15]); }
      if constexpr (p_lo + 2 < p_hi) { constexpr int e = p_lo + 2; if constexpr (e & 1) c.rs1[e >> 4] = fadd_s(c.rs1[e >> 4], c.S[X][0][e >> 4][e & 15]); else c.rs0[e >> 4] = fadd_s(c.rs0[e >> 4], c.S[X][0][e >> 4][e & 15]); }
    }
    if constexpr (e_lo + 0 < e_hi) c.S[X][0][(e_lo + 0) >> 4][(e_lo + 0) & 15] = __builtin_amdgcn_exp2f(c.S[X][0][(e_lo + 0) >> 4][(e_lo + 0) & 15]);
    if constexpr (e_lo + 1 < e_hi) c.S[X][0][(e_lo + 1) >> 4][(e_lo + 1) & 15] = __builtin_amdgcn_exp2f(c.S[X][0][(e_lo + 1) >> 4][(e_lo + 1) & 15]);
    if constexpr (e_lo + 2 < e_hi) c.S[X][0][(e_lo + 2) >> 4][(e_lo + 2) & 15] = __builtin_amdgcn_exp2f(c.S[X][0][(e_lo + 2) >> 4][(e_lo + 2) & 15]);
  }
  if constexpr (M == 31) asm volatile("" :: "v"(c.S[X][0][0]), "v"(c.S[X][0][1]), "v"(c.NEGM[0]), "v"(c.NEGM[1]), "v"(c.rs0[0]), "v"(c.rs0[1]), "v"(c.rs1[0]), "v"(c.rs1[1]));
  GAP();
}
template <int R> DI void o_scale1(float f0, float f1) {
  float t; asm volatile("v_accvgpr_read_b32 %0, a[%c2]\n\tv_mul_f32 %0, %1, %0\n\tv_accvgpr_write_b32 a[%c2], %0" : "=&v"(t) : "v"(((R >> 4) & 1) ? f1 : f0), "i"(R) : ACLOB_LO);
}
template <int... Rs> DI void o_scale(float f0, float f1, iseq<Rs...>) { (o_scale1<Rs>(f0, f1), ...); }
template <int X, bool PEEL, bool ZERO1, bool DK, bool DV, bool KR, int SJ, int... Ms> DI void phaseB_(W4C& c, const int t, iseq<Ms...>) {
  const unsigned kslot_rd = (unsigned)((t + 1) & 1) * 0x4000u;
  if constexpr (!PEEL) asm volatile("s_nop 1" ::: "memory");
  GAP();
  (gapB<X, PEEL, ZERO1, DK, DV, KR, SJ, Ms>(c, kslot_rd), ...);
#pragma unroll
  for (int e = (32 * 12) / 13; e < 32; ++e) { if (e & 1) c.rs1[e >> 4] = fadd_s(c.rs1[e >> 4], c.S[X][0][e >> 4][e & 15]); else c.rs0[e >> 4] = fadd_s(c.rs0[e >> 4], c.S[X][0][e >> 4][e & 15]); }
  if constexpr (!PEEL) {
    if (__builtin_expect(c.pend, 0)) {
      asm volatile("s_nop 7\n\ts_nop 7" ::: "memory");
      o_scale(c.fct[0], c.fct[1], make_iseq<128>{});
      asm volatile("s_nop 1" ::: "memory");
      c.lsum[0] = fmul_s(c.lsum[0], c.fct[0]); c.lsum[1] = fmul_s(c.lsum[1], c.fct[1]); c.pend = false;
    }
  }
  asm volatile("s_waitcnt lgkmcnt(0)" ::: "memory");
  GAP();
}
template <int X, bool PEEL, bool ZERO1, bool DK = true, bool DV = true, bool KR = true, int SJ = 0> DI void phaseB(W4C& c, const int t) { phaseB_<X, PEEL, ZERO1, DK, DV, KR, SJ>(c, t, make_iseq<32>{}); }
template <int... Is> DI void tr_reads(W4C& c, unsigned vslot, iseq<Is...>) { (tr_read<Is>(c, vslot), ...); }
template <int X, int... Cs> DI void packs(W4C& c, iseq<Cs...>) { (pack1<X, ((Cs >> 3) & 1), 2 * (Cs >> 4) + ((Cs >> 2) & 1), (Cs & 3)>(c), ...); }
template <int X, int J> DI void rowsum1_padded(W4C& c) { rowsum1<X, J>(c); asm volatile("s_nop 1"); }
template <int X, int... Js> DI void rowsums(W4C& c, iseq<Js...>) { (rowsum1_padded<X, Js>(c), ...); }
template <int... Ms> DI void pv_all(W4C& c, iseq<Ms...>) { (pv_mfma<Ms, false>(c), ...); }

template <int X, int KB, int E> DI void f_exp(W4C& c) { constexpr int qb = KB ? (E >> 4) : ((E >> 3) & 1), r = KB ? (E & 15) : 8 * (E >> 4) + (E & 7); c.S[X][KB][qb][r] = __builtin_amdgcn_exp2f(c.S[X][KB][qb][r]); }
template <int X, int KB, int I> DI void f_add(W4C& c) { constexpr int qb = KB ? (I >> 3) : ((I >> 2) & 1), r = KB ? 2 * (I & 7) : 8 * (I >> 3) + 2 * (I & 3);
  c.rs0[qb] = fadd_s(c.rs0[qb], c.S[X][KB][qb][r]); c.rs1[qb] = fadd_s(c.rs1[qb], c.S[X][KB][qb][r + 1]); }
template <int X, int KB, int I> DI void f_pack(W4C& c) { pack1<X, (KB ? (I >> 3) : ((I >> 2) & 1)), 2 * KB + (KB ? ((I >> 2) & 1) : (I >> 3)), (I & 3)>(c); }
template <int SL, bool FIRST, int M> DI void gapAf(W4C& c, const unsigned tk, const unsigned tv) {
  constexpr int X = SL & 1, Xp = X ^ 1, VS = ((SL + 3) & 3) * 0x4000;
  qk_mfma<X, FIRST, M>(c);
  if constexpr (M >= 16) {
    constexpr int q = (M - 16) >> 1;
    if constexpr (((M - 16) & 1) == 0) {
      if constexpr (q < 4) c.pm0[q] = c.m0k + SL * 0x4000u + 1024u * q; else c.pm0[q] = c.m0v + VS + 1024u * (q - 4);
      asm volatile("" : "+s"(c.pm0[q]));
    } else {
      if constexpr (q < 4) c.psf[q] = tk + KRS * 4u * q; else c.psf[q] = tv + KRS * 8u * ((q - 4) >> 1) + 128u * ((q - 4) & 1);
      asm volatile("" : "+s"(c.psf[q]));
    }
  }
  if constexpr (!FIRST) {
    if constexpr (M == 8) asm volatile("" :: "v"(c.NEGM[0]), "v"(c.NEGM[1]));
    if constexpr (M < 12) { tr_read<(3 * M) / 2, VS>(c, 0u); if constexpr (M & 1) tr_read<(3 * M) / 2 + 1, VS>(c, 0u); }
    else if constexpr (M < 26) tr_read<M + 6, VS>(c, 0u);
    if constexpr (M == 0) { f_add<Xp, 0, 15>(c); f_pack<Xp, 0, 15>(c); }
    f_exp<Xp, 1, M>(c);
    if constexpr (M >= 2 && (M & 1) == 0) f_add<Xp, 1, (M - 2) / 2>(c);
    if constexpr (M >= 3 && (M & 1) == 1) f_pack<Xp, 1, (M - 3) / 2>(c);
  }
  GAP();
}
template <int SL, bool FIRST, int VMA, int... Ms> DI void phaseAf_(W4C& c, const int t, iseq<Ms...>) {
  const unsigned tk = KTILEB * (unsigned)(t + 4 < NTILE ? t + 4 : NTILE - 1) + c.sofk, tv = KTILEB * (unsigned)(t + 3 < NTILE ? t + 3 : NTILE - 1) + c.sofv;
  (gapAf<SL, FIRST, Ms>(c, tk, tv), ...);
  if constexpr (FIRST) asm volatile("s_waitcnt vmcnt(%c0) lgkmcnt(0)\n\ts_barrier" :: "i"(VMA) : "memory");
  else { asm volatile("s_waitcnt vmcnt(%c16) lgkmcnt(0)" : W4_VLO : "i"(VMA) : "memory"); asm volatile("s_barrier" : W4_VUP :: "memory"); }
  GAP();
  if constexpr (!FIRST) v_concat(c);
}
template <int SL, int VMA, bool FIRST = false> DI void phaseAf(W4C& c, const int t) { phaseAf_<SL, FIRST, VMA>(c, t, make_iseq<32>{}); }
template <int SL, bool PEEL, bool ZERO1, bool DM_, bool KR, bool SJ, int M> DI void gapBf(W4C& c) {
  constexpr int X = SL & 1;
  if constexpr (!PEEL) pv_mfma<M, ZERO1>(c);
  if constexpr (M < 4 && DM_) dma16(c.pm0[M], c.vksrc[M & 1], c.srdK, c.psf[M]);
  else if constexpr (M >= 4 && M < 8 && DM_) dma16(c.pm0[M], c.vvsrc, c.srdV, c.psf[M]);
  if constexpr (SJ && M == 24) asm volatile("buffer_store_dwordx4 %0, %1, %2, %3 offen\n\ts_nop 1" : "+v"(c.sw[0]) : "v"(c.vss), "s"(c.srdSS), "s"(c.sss) : "memory");
  if constexpr (SJ && M == 26) asm volatile("buffer_store_dwordx4 %0, %1, %2, %3 offen\n\ts_nop 1" : "+v"(c.sw[1]) : "v"(c.vss), "s"(c.srdSS), "s"(c.sss + 65536u) : "memory");
  if constexpr (SJ && M == 28) asm volatile("buffer_load_dwordx4 %0, %1, %2, %3 offen" : "=v"(c.sw[0]) : "v"(c.vsl), "s"(c.srdSL), "s"(c.ssl) : "memory");
  if constexpr (SJ && M == 30) asm volatile("buffer_load_dwordx4 %0, %1, %2, %3 offen offset:32" : "=v"(c.sw[1]) : "v"(c.vsl), "s"(c.srdSL), "s"(c.ssl) : "memory");
  if constexpr (M >= 8 && M < 24 && KR) k_read<M - 8, ((SL + 1) & 3) * 0x4000>(c, 0u);
  if constexpr (!PEEL && M == 9) { f_add<X ^ 1, 1, 15>(c); f_pack<X ^ 1, 1, 15>(c); }
  f_exp<X, 0, M>(c);
  if constexpr (M >= 2 && (M & 1) == 0) f_add<X, 0, (M - 2) / 2>(c);
  if constexpr (M >= 17) f_pack<X, 0, M - 17>(c);
  GAP();
}
template <int SL, bool PEEL, bool ZERO1, bool DM_, bool KR, bool SJ, int... Ms> DI void phaseBf_(W4C& c, iseq<Ms...>) {
  constexpr int X = SL & 1;
  if constexpr (PEEL) {
    asm volatile("s_nop 7\n\ts_nop 7" ::: "memory");
#pragma unroll
    for (int q = 0; q < 2; ++q) {
      float m0 = max2f(c.S[X][0][q][0], c.S[X][1][q][0]);
#pragma unroll
      for (int e = 1; e < 16; ++e) m0 = max3f(m0, c.S[X][0][q][e], c.S[X][1][q][e]);
      auto pr = __builtin_amdgcn_permlane32_swap(__float_as_uint(m0), __float_as_uint(m0), false, false);
      const float mx = max2f(__uint_as_float(pr[0]), __uint_as_float(pr[1]));
#pragma unroll
      for (int kb = 0; kb < 2; ++kb) { _Pragma("unroll") for (int e = 0; e < 16; ++e) c.S[X][kb][q][e] = fsub_s(c.S[X][kb][q][e], mx); }
      negm_bcast(c.NEGM[q], w4_aone(), -mx);
      c.rs0[q] = 0.f; c.rs1[q] = 0.f;
    }
  } else asm volatile("s_nop 1" ::: "memory");
  GAP();
  (gapBf<SL, PEEL, ZERO1, DM_, KR, SJ, Ms>(c), ...);
  asm volatile("s_waitcnt lgkmcnt(0)" ::: "memory");
  GAP();
}
template <int SL, bool PEEL = false, bool ZERO1 = false, bool DM_ = true, bool KR = true, bool SJ = false> DI void phaseBf(W4C& c) { phaseBf_<SL, PEEL, ZERO1, DM_, KR, SJ>(c, make_iseq<32>{}); }
template <int TI, int I> DI void kv_k(W4C& c) { dma16(c.m0k + TI * 0x4000u + 1024u * I, c.vksrc[I & 1], c.srdK, TI * KTILEB + c.sofk + KRS * 4u * I); }
template <int TI, int I> DI void kv_v(W4C& c) { dma16(c.m0v + TI * 0x4000u + 1024u * I, c.vvsrc, c.srdV, TI * KTILEB + c.sofv + KRS * 8u * (I >> 1) + 128u * (I & 1)); }
template <int X, int... Es> DI void f_exps(W4C& c, iseq<Es...>) { (f_exp<X, 1, Es>(c), ...); }
template <int X, int... Is> DI void f_fin(W4C& c, iseq<Is...>) { ((f_add<X, 1, Is>(c), f_pack<X, 1, Is>(c)), ...); }
template <int... Is> DI void k_reads(W4C& c, iseq<Is...>) { (k_read<Is>(c, 0u), ...); }

struct QPC { unsigned vtr, vwsrc, m0w, sofw, vx; rsrc_t srdW, srdX; bf16x4 wl[8], wu[8]; u32x4 xv[4][8]; };
template <int F> DI void qp_tr(QPC& q, unsigned vaddr) {
  constexpr int ks = F >> 2, db = F & 3;
  asm volatile("ds_read_b64_tr_b16 %0, %1 offset:%2" : "=v"(q.wl[F & 7]) : "v"(vaddr), "i"(4096 * ks + 512 * db) : "memory");
  asm volatile("ds_read_b64_tr_b16 %0, %1 offset:%2" : "=v"(q.wu[F & 7]) : "v"(vaddr), "i"(4096 * ks + 256 + 512 * db) : "memory");
}
template <int T, int Q> DI void qp_g(QPC& q) {
  if constexpr (Q < 4) dma16(q.m0w + T * 0x4000u + 1024u * Q, q.vwsrc, q.srdW, q.sofw + T * 64u * 8192u + 8192u * 8u * (Q >> 1) + 128u * (Q & 1));
  else {
    constexpr int J = Q - 4, a = 128 + 32 * (T & 3) + 4 * J;
    if constexpr (T >= 4 && T < 8) asm volatile("buffer_load_dwordx4 %0, %1, %2, %3 offen offset:%c4" : "=v"(q.xv[T & 3][J]) : "v"(q.vx), "s"(q.srdX), "s"((unsigned)((4 * T + (J & 3)) * 65536)), "i"((J >> 2) * 1024) : "memory");
    else asm volatile("buffer_load_dwordx4 a[%c3:%c4], %0, %1, %2 offen offset:%c5" :: "v"(q.vx), "s"(q.srdX), "s"((unsigned)((4 * T + (J & 3)) * 65536)), "i"(a), "i"(a + 3), "i"((J >> 2) * 1024) : "memory", ACLOB_HI);
  }
}
template <int T, int... Q> DI void qp_grp(QPC& q, iseq<Q...>) { (qp_g<T, Q>(q), ...); }
template <int TT, int M, int LG> DI void qp_mfma(QPC& q) {
  constexpr int f = M >> 1, ks = f >> 2, db = f & 3, qb = M & 1, ao = AO_(db, qb), ax = 128 + 32 * (TT & 3) + 16 * qb + 4 * ks;
  if constexpr (qb == 0) asm volatile("s_waitcnt lgkmcnt(%c2)" : "+v"(q.wl[f & 7]), "+v"(q.wu[f & 7]) : "i"(LG) : "memory");
  const bf16x8 wf = __builtin_shufflevector(q.wl[f & 7], q.wu[f & 7], 0, 1, 2, 3, 4, 5, 6, 7);
  if constexpr (TT == 0 && ks == 0) asm volatile("v_mfma_f32_32x32x16_bf16 a[%c1:%c2], %0, a[%c3:%c4], 0" :: "v"(wf), "i"(ao), "i"(ao + 15), "i"(ax), "i"(ax + 3) : ACLOB);
  else if constexpr (TT >= 4 && TT < 8) asm volatile("v_mfma_f32_32x32x16_bf16 a[%c2:%c3], %0, %1, a[%c2:%c3]" :: "v"(wf), "v"(q.xv[TT & 3][4 * qb + ks]), "i"(ao), "i"(ao + 15) : ACLOB);
  else asm volatile("v_mfma_f32_32x32x16_bf16 a[%c1:%c2], %0, a[%c3:%c4], a[%c1:%c2]" :: "v"(wf), "i"(ao), "i"(ao + 15), "i"(ax), "i"(ax + 3) : ACLOB);
}
template <int T, int J> DI void qp_gap(QPC& q, W4C& c, unsigned vrd) {
  if constexpr (J < 8) { if constexpr (T >= 0) qp_mfma<T, 24 + J, (T == 9 ? 0 : 6)>(q); }
  else qp_mfma<T + 1, J - 8, 6>(q);
  if constexpr (T < 9 && (J & 1) == 0) qp_tr<(J >> 1)>(q, vrd);
  if constexpr (T <= 1 && J < 24) qp_g<2 * T + 4 + J / 12, J % 12>(q);
  if constexpr ((T == 2 || T == 3) && (J & 1) && J < 24) qp_g<T + 6, (J >> 1)>(q);
  if constexpr (T == 4 && (J & 1) && J < 24) kv_k<(J >> 3), (J >> 1) & 3>(c);
  if constexpr (T == 5 && (J & 1) && J < 8) kv_k<3, (J >> 1)>(c);
  if constexpr (T >= 5 && T <= 7 && (J & 1) && J >= 8 && J < 16) kv_v<T - 5, (J >> 1) & 3>(c);
  if constexpr (T == 9 && J >= 6) { k_read<8 * (J - 6)>(c, 0u); k_read<8 * (J - 6) + 1>(c, 0u); k_read<8 * (J - 6) + 2>(c, 0u); k_read<8 * (J - 6) + 3>(c, 0u);
                                    k_read<8 * (J - 6) + 4>(c, 0u); k_read<8 * (J - 6) + 5>(c, 0u); k_read<8 * (J - 6) + 6>(c, 0u); k_read<8 * (J - 6) + 7>(c, 0u); }
  GAP();
}
template <int T, int VM, int... Js> DI void qp_step_(QPC& q, W4C& c, iseq<Js...>) {
  const unsigned vrd = q.vtr + (T + 1) * 0x4000u;
  constexpr int u = (T + 1) & 3;
  if constexpr (T + 1 >= 4 && T + 1 < 8) asm volatile("s_waitcnt vmcnt(%c8)\n\ts_barrier" : "+v"(q.xv[u][0]), "+v"(q.xv[u][1]), "+v"(q.xv[u][2]), "+v"(q.xv[u][3]), "+v"(q.xv[u][4]), "+v"(q.xv[u][5]), "+v"(q.xv[u][6]), "+v"(q.xv[u][7]) : "i"(VM) : "memory");
  else if constexpr (T < 9) asm volatile("s_waitcnt vmcnt(%c0)\n\ts_barrier" :: "i"(VM) : "memory");
  GAP();
  (qp_gap<T, Js>(q, c, vrd), ...);
}
template <int T, int VM> DI void qp_step(QPC& q, W4C& c) { qp_step_<T, VM>(q, c, make_iseq<(T == 9 ? 8 : 32)>{}); }
template <int R> DI u32x4 acc_pack8(float f, float* chk = nullptr) {
  float o[8];
  asm volatile("v_accvgpr_read_b32 %0, a[%c8]\n\tv_accvgpr_read_b32 %1, a[%c9]\n\tv_accvgpr_read_b32 %2, a[%c10]\n\tv_accvgpr_read_b32 %3, a[%c11]\n\t"
               "v_accvgpr_read_b32 %4, a[%c12]\n\tv_accvgpr_read_b32 %5, a[%c13]\n\tv_accvgpr_read_b32 %6, a[%c14]\n\tv_accvgpr_read_b32 %7, a[%c15]"
               : "=v"(o[0]), "=v"(o[1]), "=v"(o[2]), "=v"(o[3]), "=v"(o[4]), "=v"(o[5]), "=v"(o[6]), "=v"(o[7])
               : "i"(R), "i"(R + 1), "i"(R + 2), "i"(R + 3), "i"(R + 4), "i"(R + 5), "i"(R + 6), "i"(R + 7));
  u32x4 w;
#pragma unroll
  for (int e = 0; e < 4; ++e) w[e] = cvtpk(o[2 * e] * f, o[2 * e + 1] * f);
  if (chk) {
#pragma unroll
    for (int e = 0; e < 8; ++e) asm("v_fmac_f32_e32 %0, 0, %1" : "+v"(chk[e & 1]) : "v"(o[e]));
  }
  return w;
}
template <int I> DI void q_conv1(float C) {
  constexpr int qb = I >> 3, d0 = I & 7, aq = AQ_(qb, d0);
  const u32x4 w = acc_pack8<AO_(d0 >> 1, qb) + 8 * (d0 & 1)>(C);
  asm volatile("v_accvgpr_write_b32 a[%c4], %0\n\tv_accvgpr_write_b32 a[%c5], %1\n\tv_accvgpr_write_b32 a[%c6], %2\n\tv_accvgpr_write_b32 a[%c7], %3"
               :: "v"(w[0]), "v"(w[1]), "v"(w[2]), "v"(w[3]), "i"(aq), "i"(aq + 1), "i"(aq + 2), "i"(aq + 3) : ACLOB_HI);
}
template <int... Is> DI void q_conv(float C, iseq<Is...>) { (q_conv1<Is>(C), ...); }
template <int I> DI void o_store1(unsigned char* dst, float il0, float il1, float* chk) {
  constexpr int qb = I >> 3, j = I & 7;
  const u32x4 w = acc_pack8<AO_(j >> 1, qb) + 8 * (j & 1)>(qb ? il1 : il0, chk);
  *(u32x4*)(dst + qb * 1024u + (size_t)j * 65536u) = w;
}
template <int... Is> DI void o_store(unsigned char* dst, float il0, float il1, float* chk, iseq<Is...>) { (o_store1<Is>(dst, il0, il1, chk), ...); }
DI void gload16(u32x4& r, const void* p) { asm volatile("global_load_dwordx4 %0, %1, off" : "=v"(r) : "v"(p) : "memory"); }
DI void gstore16(void* p, const u32x4& r) { asm volatile("global_store_dwordx4 %0, %1, off\n\ts_nop 1" :: "v"(p), "v"(r) : "memory"); }

__global__ __launch_bounds__(256, 1)
void k_attn(const unsigned char* __restrict__ xnF, const unsigned short* __restrict__ wq, const unsigned short* __restrict__ Kc, const unsigned short* __restrict__ Vc,
            unsigned char* __restrict__ OF, const unsigned char* __restrict__ zF, unsigned char* __restrict__ z2nT,
            const unsigned short* __restrict__ w_sp, unsigned char* __restrict__ wspF,
            const unsigned short* __restrict__ wo, const unsigned short* __restrict__ w_out) {
  extern __shared__ __attribute__((aligned(16))) char lds[];
  const unsigned tid = threadIdx.x, lane = tid & 63u, wid = rfl(tid >> 6), l32 = lane & 31u, hi = lane >> 5;
  constexpr int T = NTILE;
  const float C = 0.12751743082f;
  const unsigned xcd = blockIdx.x & 7u, idx = blockIdx.x >> 3;
  const unsigned h = 8u * (xcd >> 1) + (idx & 7u), y = 4u * (xcd & 1u) + (idx >> 3);
  const unsigned rb0 = 8u * y + 2u * wid;
  W4C c; QPC q;
  TS(2, 0);
  c.ldsb = (unsigned)(size_t)lds;
#pragma unroll
  for (int j = 0; j < 4; ++j) c.vkb[j] = c.ldsb + KRING + 256u * l32 + 16u * ((2u * j + hi) ^ (l32 & 7u));
  c.vvb = c.ldsb + VRING + tr_lane<false, false>(lane, 0u);
#pragma unroll
  for (int par = 0; par < 2; ++par) c.vksrc[par] = KRS * (lane >> 4) + 16u * ((lane & 15u) ^ (4u * par + (lane >> 4)));
  c.vvsrc = KRS * ((lane >> 2) & 7u) + 64u * hi + 16u * (lane & 3u);
  c.m0k = rfl(c.ldsb + KRING + 4096u * wid); c.m0v = rfl(c.ldsb + VRING + 4096u * wid);
  c.sofk = KRS * 16u * wid; c.sofv = KRS * 16u * wid;
  c.srdK = mk_srd((const char*)Kc + (size_t)h * SEQ * 256u, SEQ * 256u);
  c.srdV = mk_srd((const char*)Vc + (size_t)h * SEQ * 256u, SEQ * 256u);
  q.vtr = c.ldsb + WRING + tr_lane<true, true>(lane, 2048u);
  q.vwsrc = 8192u * ((lane >> 2) & 7u) + 64u * hi + 16u * (lane & 3u);
  q.m0w = rfl(c.ldsb + WRING + 4096u * wid); q.sofw = 8192u * 16u * wid;
  q.vx = rb0 * 1024u + lane * 16u;
  q.srdW = mk_srd((const char*)wq + h * 256u, DM * 8192u - h * 256u);
  q.srdX = mk_srd(xnF, SEQ * DM * 2u);
  const unsigned srow = blockIdx.x * 8u + 2u * wid + hi;
  u32x4 zv[4];
#pragma unroll
  for (int i = 0; i < 4; ++i) { const unsigned pc = l32 + 32u * i, p = pc < 108u ? pc : 107u;
    gload16(zv[i], zF + ((size_t)((54u + (p >> 1)) * 64u + (srow >> 5)) * 64u + (p & 1u) * 32u + (srow & 31u)) * 16u); }
  asm volatile("s_nop 4" ::: "memory");
  qp_grp<0>(q, make_iseq<12>{}); qp_grp<1>(q, make_iseq<12>{});
  asm volatile("s_waitcnt vmcnt(24)" : "+v"(zv[0]), "+v"(zv[1]), "+v"(zv[2]), "+v"(zv[3]) :: "memory");
  {
    const bool v3 = l32 < 12u;
    float f[32], s = 0.f;
#pragma unroll
    for (int i = 0; i < 4; ++i)
#pragma unroll
      for (int e = 0; e < 4; ++e) { f[8 * i + 2 * e] = bflo(zv[i][e]); f[8 * i + 2 * e + 1] = bfhi(zv[i][e]); const float t2 = f[8 * i + 2 * e] + f[8 * i + 2 * e + 1]; s += (i < 3 || v3) ? t2 : 0.f; }
#pragma unroll
    for (int o = 16; o; o >>= 1) s += __shfl_xor(s, o);
    const float mean = s * (1.f / FH); float qq = 0.f;
#pragma unroll
    for (int i = 0; i < 32; ++i) { f[i] -= mean; qq += (i < 24 || v3) ? f[i] * f[i] : 0.f; }
#pragma unroll
    for (int o = 16; o; o >>= 1) qq += __shfl_xor(qq, o);
    const float rstd = 1.f / sqrtf(qq * (1.f / FH) + 1e-5f);
#pragma unroll
    for (int i = 0; i < 4; ++i) { const unsigned pc = l32 + 32u * i, p = pc < 108u ? pc : 107u;
      u32x4 w;
#pragma unroll
      for (int e = 0; e < 4; ++e) w[e] = cvtpk(f[8 * i + 2 * e] * rstd, f[8 * i + 2 * e + 1] * rstd);
      gstore16(z2nT + ((size_t)(blockIdx.x * 27u + (p >> 2)) * 8u + (srow & 7u)) * 64u + (p & 3u) * 16u, w); }
  }
  TS(2, 4); TS(2, 5);
  asm volatile("s_waitcnt lgkmcnt(0)" ::: "memory");
  qp_step<-1, 16>(q, c); qp_step<0, 28>(q, c); qp_step<1, 36>(q, c); qp_step<2, 48>(q, c); qp_step<3, 48>(q, c); qp_step<4, 48>(q, c);
  qp_step<5, 48>(q, c); qp_step<6, 44>(q, c); qp_step<7, 36>(q, c); qp_step<8, 24>(q, c); qp_step<9, 0>(q, c);
  asm volatile("s_nop 7\n\ts_nop 7" ::: "memory");
  q_conv(C, make_iseq<16>{});
  asm volatile("s_waitcnt lgkmcnt(0)\n\ts_nop 1" ::: "memory");
  GAP();
  TS(2, 1);
  { const unsigned srb = blockIdx.x >> 2, ks0 = (blockIdx.x & 3u) * 32u + wid * 8u;
    c.vsl = (srb * 32u + l32) * 4096u + ks0 * 32u + hi * 16u; c.vss = (ks0 * 64u + srb) * 1024u + lane * 16u;
    c.srdSL = mk_srd(w_sp, SEQ * SEQ * 2u); c.srdSS = mk_srd(wspF, SEQ * SEQ * 2u); c.sw[0] = c.sw[1] = (u32x4){0u, 0u, 0u, 0u}; }
  phaseAf<0, 8, true>(c, 0); phaseBf<0, true>(c);
  phaseAf<1, 12>(c, 1); phaseBf<1, false, true>(c);
  { const unsigned gw = blockIdx.x * 4u + wid;
    const bool isw = gw < 640u; const unsigned go = isw ? gw : (gw < 775u ? gw - 640u : 134u);
    const rsrc_t srdP = mk_srd(isw ? wo : w_out, isw ? NH * DH * DM * 2u : FH * DM * 2u);
    asm volatile("s_mov_b32 m0, %0\n\ts_nop 4\n\tbuffer_load_dword %1, %2, %3 offen lds\n\tbuffer_load_dword %1, %2, %3 offen offset:64 lds"
                 :: "s"(c.ldsb + 0x20000u + wid * 1024u), "v"(lane * 128u), "s"(srdP), "s"(go * 8192u) : "m0", "memory"); }
  for (int t = 2; t < T - 2; t += 4) {
    const unsigned fi = (unsigned)(t >> 2), fl = fi < 3u ? fi : 3u, fs = fi == 0u ? 0u : (fi - 1u < 3u ? fi - 1u : 3u);
    c.ssl = fl * 64u; c.sss = fs * 131072u;
    phaseAf<2, 16>(c, t);     phaseBf<2, false, false, true, true, true>(c);
    phaseAf<3, 20>(c, t + 1); phaseBf<3>(c);
    phaseAf<0, 20>(c, t + 2); phaseBf<0>(c);
    phaseAf<1, 20>(c, t + 3); phaseBf<1>(c);
  }
  TS(2, 2);
  phaseAf<2, 16>(c, T - 2); phaseBf<2, false, false, false, true>(c);
  phaseAf<3, 0>(c, T - 1);  phaseBf<3, false, false, false, false>(c);
  asm volatile("" :: "v"(c.sw[0]), "v"(c.sw[1]));
  unsigned lb; asm volatile("v_mbcnt_lo_u32_b32 %0, -1, 0\n\tv_mbcnt_hi_u32_b32 %0, -1, %0" : "=v"(lb));
  unsigned char* const odst = OF + ((size_t)(8u * h * 64u + rb0) * 64u + lb) * 16u;
  bool bad;
  {
    tr_reads(c, 3u * 0x4000u, make_iseq<32>{});
    f_add<1, 0, 15>(c); f_pack<1, 0, 15>(c);
    f_exps<1>(c, make_iseq<32>{});
    f_fin<1>(c, make_iseq<16>{});
    asm volatile("s_waitcnt lgkmcnt(0)" : W4_VLO :: "memory"); asm volatile("" : W4_VUP :: "memory");
    GAP();
    v_concat(c);
    asm volatile("s_nop 1" ::: "memory");
    pv_all(c, make_iseq<32>{});
    float l[2];
#pragma unroll
    for (int qb = 0; qb < 2; ++qb) { const float rs = c.rs0[qb] + c.rs1[qb];
      auto pr = __builtin_amdgcn_permlane32_swap(__float_as_uint(rs), __float_as_uint(rs), false, false); l[qb] = __uint_as_float(pr[0]) + __uint_as_float(pr[1]); }
    asm volatile("s_nop 7\n\ts_nop 7" ::: "memory");
    float chk[2] = {0.f, 0.f};
    o_store(odst, __builtin_amdgcn_rcpf(l[0]), __builtin_amdgcn_rcpf(l[1]), chk, make_iseq<16>{});
    const bool ok = (l[0] < 0x1p64f) & (l[1] < 0x1p64f) & (chk[0] == 0.f) & (chk[1] == 0.f);
    volatile unsigned* flag = (volatile unsigned*)(lds + 0x22000);
    const unsigned wbad = __any((int)!ok) ? 1u : 0u;
    if (lb == 0u) flag[wid] = wbad;
    __syncthreads();
    bad = rfl(flag[0] | flag[1] | flag[2] | flag[3]) != 0u;
  }
  if (__builtin_expect(bad, 0)) {
    asm volatile("s_waitcnt vmcnt(0) lgkmcnt(0)\n\ts_nop 4" ::: "memory");
#pragma unroll
    for (int i = 0; i < 4; ++i) {
      dma16(c.m0k + 1024u * i, c.vksrc[i & 1], c.srdK, c.sofk + KRS * 4u * i);
      dma16(c.m0v + 1024u * i, c.vvsrc, c.srdV, c.sofv + KRS * 8u * (i >> 1) + 128u * (i & 1));
      dma16(c.m0k + 0x4000u + 1024u * i, c.vksrc[i & 1], c.srdK, KTILEB + c.sofk + KRS * 4u * i);
    }
    asm volatile("s_waitcnt vmcnt(0)\n\ts_barrier" ::: "memory");
    k_reads(c, make_iseq<16>{});
    asm volatile("s_waitcnt lgkmcnt(0)" ::: "memory");
    GAP();
    c.mhat[0] = c.mhat[1] = 0.f; c.lsum[0] = c.lsum[1] = 0.f; c.fct[0] = c.fct[1] = 1.f; c.pend = false;
    phaseA<0, true>(c, 0);  phaseB<0, true, false>(c, 0);
    phaseA<1, false>(c, 1); phaseB<1, false, true>(c, 1);
    for (int t = 2; t < T - 2; t += 2) {
      phaseA<0, false>(c, t);     phaseB<0, false, false>(c, t);
      phaseA<1, false>(c, t + 1); phaseB<1, false, false>(c, t + 1);
    }
    phaseA<0, false>(c, T - 2); phaseB<0, false, false, false, true, true>(c, T - 2);
    phaseA<1, false>(c, T - 1); phaseB<1, false, false, false, false, false>(c, T - 1);
    tr_reads(c, (unsigned)((T - 1) & 1) * 0x4000u, make_iseq<32>{});
#pragma unroll
    for (int e = 0; e < 32; ++e) c.S[1][1][e >> 4][e & 15] = __builtin_amdgcn_exp2f(c.S[1][1][e >> 4][e & 15]);
    packs<1>(c, make_iseq<32>{});
    rowsums<1>(c, iseq<8, 9, 10, 11, 12, 13, 14, 15, 24, 25, 26, 27, 28, 29, 30, 31>{});
    lsum_merge(c);
    asm volatile("s_waitcnt lgkmcnt(0)" : W4_VLO :: "memory"); asm volatile("" : W4_VUP :: "memory");
    GAP();
    v_concat(c);
    asm volatile("s_nop 1" ::: "memory");
    pv_all(c, make_iseq<32>{});
    asm volatile("s_nop 7\n\ts_nop 7" ::: "memory");
    unsigned lb2; asm volatile("v_mbcnt_lo_u32_b32 %0, -1, 0\n\tv_mbcnt_hi_u32_b32 %0, -1, %0" : "=v"(lb2));
    o_store(OF + ((size_t)(8u * h * 64u + rb0) * 64u + lb2) * 16u, __builtin_amdgcn_rcpf(c.lsum[0]), __builtin_amdgcn_rcpf(c.lsum[1]), nullptr, make_iseq<16>{});
  }
  TS(2, 3);
}

extern "C" void kernel_launch(void* const* d_in, const int* in_sizes, int n_in, void* d_out, int out_size, void* d_ws, size_t ws_size, hipStream_t stream) {
  const unsigned short* x = (const unsigned short*)d_in[0];
  const unsigned short* Kc = (const unsigned short*)d_in[1];
  const unsigned short* Vc = (const unsigned short*)d_in[2];
  const unsigned short* wq = (const unsigned short*)d_in[3];
  const unsigned short* wo = (const unsigned short*)d_in[4];
  const unsigned short* w_in = (const unsigned short*)d_in[6];
  const unsigned short* w_sp = (const unsigned short*)d_in[7];
  const unsigned short* b_sp = (const unsigned short*)d_in[8];
  const unsigned short* w_out = (const unsigned short*)d_in[9];
  unsigned char* ws = (unsigned char*)d_ws;
  (void)hipFuncSetAttribute((const void*)k_attn, hipFuncAttributeMaxDynamicSharedMemorySize, 163840);
  (void)hipFuncSetAttribute((const void*)k_zgemm, hipFuncAttributeMaxDynamicSharedMemorySize, 163840);
  (void)hipFuncSetAttribute((const void*)k_mid, hipFuncAttributeMaxDynamicSharedMemorySize, 163840);
  (void)hipFuncSetAttribute((const void*)k_out, hipFuncAttributeMaxDynamicSharedMemorySize, 110592);
  hipLaunchKernelGGL(k_ln, dim3(516), dim3(256), 0, stream, x, ws + WS_XN, w_in, wq);
  hipLaunchKernelGGL(k_zgemm, dim3(224), dim3(256), 163840, stream, ws + WS_XN, w_in, ws + WS_Z);
  hipLaunchKernelGGL(k_attn, dim3(256), dim3(256), 163840, stream, ws + WS_XN, wq, Kc, Vc, ws + WS_O, ws + WS_Z, ws + WS_Z2N, w_sp, ws + WS_WSP, wo, w_out);
  hipLaunchKernelGGL(k_mid, dim3(256), dim3(256), 163840, stream, ws + WS_O, wo, ws + WS_SP, ws + WS_WSP, ws + WS_Z2N, ws + WS_Z, b_sp, ws + WS_U);
  hipLaunchKernelGGL(k_out, dim3(160), dim3(256), 110592, stream, ws + WS_U, w_out, ws + WS_SP, x, (unsigned short*)d_out);
}
```
